```python
import torch

def _softmax(x, axis=-1):
    return torch.softmax(x, dim=axis)
def _gelu(x):
    return torch.nn.functional.gelu(x, approximate="tanh")
def _silu(x):
    return torch.nn.functional.silu(x)
def _sigmoid(x):
    return torch.sigmoid(x)
def _layernorm(x, axis=-1, eps=1e-5):
    m = x.mean(dim=axis, keepdim=True)
    v = x.var(dim=axis, keepdim=True, unbiased=False)
    return (x - m) / torch.sqrt(v + eps)
def _scatter_add(tgt, idx, src, axis=0):
    return tgt.clone().index_add_(axis, idx, src)
def _segment_sum(src, seg, n):
    out = torch.zeros((n,) + src.shape[1:], dtype=src.dtype)
    return out.index_add_(0, seg, src)
def _pad(x, pw):
    flat = tuple(v for pair in reversed(pw) for v in pair)
    return torch.nn.functional.pad(x, flat)

kernel_name = 'sequential_preLN__sliding_local_dilated__mixer_token_mlp__prompt'

def setup_inputs():

    _w = (lambda a: (lambda t: t.to(torch.bfloat16) if t.is_floating_point() else t)(torch.as_tensor(a)))
    def _A_setup():
        B, S, D = 1, 2048, 512
        return {
            "x": _w((lambda *s: torch.randn(*s, dtype=torch.bfloat16) * 0.05)(B, S, D)),
            "wqkv": _w((lambda *s: torch.randn(*s, dtype=torch.bfloat16) * 0.05)(D, 3 * D)),
            "wo": _w((lambda *s: torch.randn(*s, dtype=torch.bfloat16) * 0.05)(D, D)),
        }
    _A_in = _A_setup()

    def _B_setup():
        B, S, D, F = 1, 2048, 512, 1792
        return {
            "x": _w((lambda *s: torch.randn(*s, dtype=torch.bfloat16) * 0.05)(B, S, D)),
            "w1": _w((lambda *s: torch.randn(*s, dtype=torch.bfloat16) * 1.6)(S, F) * (S ** -0.5)),
            "b1": _w((lambda *s: torch.randn(*s, dtype=torch.bfloat16) * 12.8)(F) * 0.02),
            "w2": _w((lambda *s: torch.randn(*s, dtype=torch.bfloat16) * 0.8)(F, S) * (F ** -0.5)),
            "b2": _w((lambda *s: torch.randn(*s, dtype=torch.bfloat16) * 12.8)(S) * 0.02),
        }
    _B_in = _B_setup()

    return {
        "A_x": _A_in["x"],
        "A_wqkv": _A_in["wqkv"],
        "A_wo": _A_in["wo"],
        "B_x": _B_in["x"],
        "B_w1": _B_in["w1"],
        "B_b1": _B_in["b1"],
        "B_w2": _B_in["w2"],
        "B_b2": _B_in["b2"],
    }

def reference(A_x, A_wqkv, A_wo, B_x, B_w1, B_b1, B_w2, B_b2):
    def _A_ref(x, wqkv, wo):
        B, S, D = x.shape
        H = 32; Dh = D // H
        W, dil = 8, 2
        qkv = (lambda x, *d: x.permute(*d))((x @ wqkv).reshape(B, S, 3, H, Dh), 2, 0, 3, 1, 4)
        q, k, v = qkv[0], qkv[1], qkv[2]
        scale = Dh ** -0.5
        pos = torch.arange(S)
        off = torch.arange(W) * dil
        idx = pos[:, None] - off[None, :]
        mask = idx >= 0
        idx_c = torch.where(mask, idx, 0).reshape(-1)
        kg = k[:, :, idx_c, :].reshape(B, H, S, W, Dh)
        vg = v[:, :, idx_c, :].reshape(B, H, S, W, Dh)
        logits = torch.einsum("bhsd,bhswd->bhsw", q, kg) * scale
        logits = torch.where(mask[None, None, :, :], logits, -1e9)
        attn = _softmax(logits, axis=-1)
        o = torch.einsum("bhsw,bhswd->bhsd", attn, vg)
        return (lambda x, *d: x.permute(*d))(o, 0, 2, 1, 3).reshape(B, S, D) @ wo

    def _B_ref(x, w1, b1, w2, b2):
        B, S, D = x.shape
        h = _layernorm(x, axis=-1)
        u = torch.einsum("bsd,sf->bdf", h, w1) + b1
        u = _gelu(u)
        y = torch.einsum("bdf,fs->bds", u, w2) + b2
        return (lambda x, *d: x.permute(*d))(y, 0, 2, 1)

    x = A_x
    h = x + _A_ref(_layernorm(x, axis=-1), A_wqkv, A_wo)
    h2 = h + _B_ref(_layernorm(h, axis=-1), B_w1, B_b1, B_w2, B_b2)
    return h2
```

```cpp
#include <hip/hip_runtime.h>
#include <hip/hip_bf16.h>
typedef unsigned short u16;
__device__ __forceinline__ float b2f(u16 v){ return __uint_as_float(((unsigned)v)<<16); }
__device__ __forceinline__ u16 f2b(float f){ unsigned u=__float_as_uint(f); u+=0x7FFF+((u>>16)&1); return (u16)(u>>16);}
constexpr int S=2048,D=512,F=1792;
__global__ void k_ln(const u16* x, float* xn, int twice){
  int s=blockIdx.x, l=threadIdx.x; float v[8]; float sum=0;
  for(int i=0;i<8;i++){v[i]=b2f(x[s*D+l*8+i]); sum+=v[i];}
  for(int it=0; it<=twice; ++it){
    sum=0; for(int i=0;i<8;i++) sum+=v[i];
    for(int o=32;o;o>>=1) sum+=__shfl_xor(sum,o);
    float m=sum/D, q=0; for(int i=0;i<8;i++){q+=(v[i]-m)*(v[i]-m);}
    for(int o=32;o;o>>=1) q+=__shfl_xor(q,o);
    float r=rsqrtf(q/D+1e-5f);
    for(int i=0;i<8;i++) v[i]=b2f(f2b((v[i]-m)*r));
  }
  for(int i=0;i<8;i++) xn[s*D+l*8+i]=v[i];
}
__global__ void k_qkv(const float* xn, const u16* w, float* qkv){ int n=blockIdx.x*256+threadIdx.x, s=blockIdx.y; float a=0; for(int k=0;k<D;k++) a+=xn[s*D+k]*b2f(w[k*1536+n]); qkv[s*1536+n]=b2f(f2b(a)); }
__global__ void k_attn(const float* qkv, float* o){ int s=blockIdx.x, h=threadIdx.x; if(h>=32) return; float lg[8]; float mx=-1e30f;
  for(int w=0;w<8;w++){int t=s-2*w; float a=0; if(t>=0){ for(int d=0;d<16;d++) a+=qkv[s*1536+h*16+d]*qkv[t*1536+512+h*16+d]; a*=0.25f;} else a=-1e9f; lg[w]=a; mx=fmaxf(mx,a);}
  float sum=0; for(int w=0;w<8;w++){lg[w]=__expf(lg[w]-mx); sum+=lg[w];}
  for(int d=0;d<16;d++){ float a=0; for(int w=0;w<8;w++){int t=s-2*w; if(t<0)t=0; a+=lg[w]/sum*qkv[t*1536+1024+h*16+d];} o[s*D+h*16+d]=b2f(f2b(a)); } }
__global__ void k_wo(const float* o, const u16* wo, const u16* x, u16* h){ int n=blockIdx.x*256+threadIdx.x, s=blockIdx.y; float a=0; for(int k=0;k<D;k++) a+=o[s*D+k]*b2f(wo[k*D+n]); h[s*D+n]=f2b(b2f(f2b(a))+b2f(x[s*D+n])); }
__global__ void k_u(const float* hn, const u16* w1, const u16* b1, float* u){ int f=blockIdx.x*256+threadIdx.x, d=blockIdx.y; float a=0; for(int s=0;s<S;s++) a+=hn[s*D+d]*b2f(w1[s*F+f]); a=b2f(f2b(b2f(f2b(a))+b2f(b1[f]))); float t=tanhf(0.7978845608f*(a+0.044715f*a*a*a)); u[d*F+f]=b2f(f2b(0.5f*a*(1.f+t))); }
__global__ void k_y(const float* u, const u16* w2, const u16* b2, const u16* h, u16* out){ int s=blockIdx.x*256+threadIdx.x, d=blockIdx.y; float a=0; for(int f=0;f<F;f++) a+=u[d*F+f]*b2f(w2[f*S+s]); a=b2f(f2b(b2f(f2b(a))+b2f(b2[s]))); out[s*D+d]=f2b(a+b2f(h[s*D+d])); }
extern "C" void kernel_launch(void* const* d_in, const int* in_sizes, int n_in, void* d_out, int out_size, void* d_ws, size_t ws_size, hipStream_t stream){
  const u16* x=(const u16*)d_in[0]; const u16* wqkv=(const u16*)d_in[1]; const u16* wo=(const u16*)d_in[2];
  const u16* w1=(const u16*)d_in[4]; const u16* b1=(const u16*)d_in[5]; const u16* w2=(const u16*)d_in[6]; const u16* b2=(const u16*)d_in[7];
  char* ws=(char*)d_ws; float* xn=(float*)ws; float* qkv=(float*)(ws+(8<<20)); float* o=(float*)(ws+(24<<20)); u16* h=(u16*)(ws+(32<<20)); float* hn=(float*)(ws+(40<<20)); float* u=(float*)(ws+(48<<20));
  k_ln<<<S,64,0,stream>>>(x,xn,0);
  k_qkv<<<dim3(6,S),256,0,stream>>>(xn,wqkv,qkv);
  k_attn<<<S,64,0,stream>>>(qkv,o);
  k_wo<<<dim3(2,S),256,0,stream>>>(o,wo,x,h);
  k_ln<<<S,64,0,stream>>>(h,hn,1);
  k_u<<<dim3(7,D),256,0,stream>>>(hn,w1,b1,u);
  k_y<<<dim3(8,D),256,0,stream>>>(u,w2,b2,h,(u16*)d_out);
}
```

```cpp
#include <hip/hip_runtime.h>
#include <hip/hip_bf16.h>

typedef unsigned u32;
typedef unsigned long long u64;
typedef u32 v4u __attribute__((ext_vector_type(4)));
typedef u32 v2u __attribute__((ext_vector_type(2)));
typedef float f4 __attribute__((ext_vector_type(4)));
typedef float f2 __attribute__((ext_vector_type(2)));
typedef __bf16 v8bf __attribute__((ext_vector_type(8)));
typedef __bf16 v2bf __attribute__((ext_vector_type(2)));
typedef short v4s __attribute__((ext_vector_type(4)));
#define LAS __attribute__((address_space(3)))
#define DI __device__ __forceinline__

#define STAMPS 0

constexpr int S_ = 2048, D_ = 512, F_ = 1792;
constexpr u32 WS_EPOCH = 0, WS_FLAGA = 4096, WS_FLAGD = WS_FLAGA + 32768, WS_FLAGC = WS_FLAGD + 32768, WS_STATS = WS_FLAGC + 32768;
constexpr u32 WS_STAMP = 512 * 1024;
constexpr u32 WS_OF = 1u << 20, WS_HF = 3u << 20, WS_UF = 5u << 20, WS_BYTES = 8u << 20;
constexpr int REPC = 16;
constexpr u32 STG = 34816, L_MISC1 = 152 * 1024, L_M = 144 * 1024, LDS_BYTES = 160 * 1024;
constexpr u32 SPIN_CAP = 1u << 16;
constexpr u32 CODE_BYTES = 28000;

DI v4u mkrs(const void* p, u32 bytes) {
  v4u r;
  r.x = (u32)(size_t)p;
  r.y = (u32)((size_t)p >> 32) & 0xffffu;
  r.z = bytes;
  r.w = 0x00020000u;
  return r;
}
#define dma1(rs, voff, soff, m0b, imm) \
  asm volatile("s_add_u32 m0, %3, %4\n\ts_nop 0\n\tbuffer_load_dwordx4 %0, %1, %2 offen lds" ::"v"(voff), "s"(rs), "s"(soff), "s"(m0b), "i"(imm) : "memory", "scc")
#define dma2(rs, voffa, voffb, soff, m0b, imm)                                                                                                        \
  asm volatile("s_add_u32 m0, %4, %5\n\ts_nop 0\n\tbuffer_load_dwordx4 %0, %2, %3 offen lds\n\tbuffer_load_dwordx4 %1, %2, %3 offen offset:1024 lds" ::"v"(voffa), \
               "v"(voffb), "s"(rs), "s"(soff), "s"(m0b), "i"(imm)                                                                                     \
               : "memory", "scc")
#define dma3(rs, voff, soff, m0b, imm)                                                                                                               \
  asm volatile(                                                                                                                                      \
      "s_add_u32 m0, %3, %4\n\ts_nop 0\n\tbuffer_load_dwordx4 %0, %1, %2 offen lds\n\tbuffer_load_dwordx4 %0, %1, %2 offen offset:1024 lds\n\tbuffer_load_dwordx4 %0, %1, %2 offen offset:2048 lds" ::"v"(voff), \
      "s"(rs), "s"(soff), "s"(m0b), "i"(imm)                                                                                                         \
      : "memory", "scc")
#define LD_SC1(dst, rs, voff, soff, imm) asm volatile("buffer_load_dwordx4 %0, %1, %2, %3 offen offset:%4 sc1" : "=v"(dst) : "v"(voff), "s"(rs), "s"(soff), "i"(imm) : "memory")
#define WAITV(n) asm volatile("s_waitcnt vmcnt(%0)" ::"i"(n) : "memory")
#define WAITV1(n, a) asm volatile("s_waitcnt vmcnt(%1)" : "+v"(a) : "i"(n) : "memory")
#define WAITV2(n, a, b) asm volatile("s_waitcnt vmcnt(%2)" : "+v"(a), "+v"(b) : "i"(n) : "memory")
#define BAR() asm volatile("s_waitcnt lgkmcnt(0)\n\ts_barrier" ::: "memory")
DI void st_sc1_128(v4u rs, u32 voff, v4u v) { asm volatile("buffer_store_dwordx4 %0, %1, %2, 0 offen sc1\n\ts_nop 1" ::"v"(v), "v"(voff), "s"(rs) : "memory"); }
DI void st_sc1_32(v4u rs, u32 voff, u32 v) { asm volatile("buffer_store_dword %0, %1, %2, 0 offen sc1" ::"v"(v), "v"(voff), "s"(rs) : "memory"); }

DI f4 mfma32(v4u a, v4u b, f4 c) { return __builtin_amdgcn_mfma_f32_16x16x32_bf16(__builtin_bit_cast(v8bf, a), __builtin_bit_cast(v8bf, b), c, 0, 0, 0); }
DI f4 mfma16(v2u a, v2u b, f4 c) { return __builtin_amdgcn_mfma_f32_16x16x16bf16_1k(__builtin_bit_cast(v4s, a), __builtin_bit_cast(v4s, b), c, 0, 0, 0); }
DI u32 pk(float a, float b) {
  f2 v = {a, b};
  return __builtin_bit_cast(u32, __builtin_convertvector(v, v2bf));
}
DI float blo(u32 v) { return __uint_as_float(v << 16); }
DI float bhi(u32 v) { return __uint_as_float(v & 0xffff0000u); }
DI float dot2(u32 a, u32 b, float c) { return __builtin_amdgcn_fdot2_f32_bf16(__builtin_bit_cast(v2bf, a), __builtin_bit_cast(v2bf, b), c, false); }
DI v4u lds128(u32 a) { return *(LAS v4u*)a; }
DI f4 lds128f(u32 a) { return *(LAS f4*)a; }
DI v2u trd(u32 a) { return __builtin_bit_cast(v2u, __builtin_amdgcn_ds_read_tr16_b64_v4i16((LAS v4s*)a)); }
DI v4u trfrag(u32 a) {
  v2u x = trd(a), y = trd(a + 512);
  v4u r = {x.x, x.y, y.x, y.y};
  return r;
}
template <int CTRL> DI float dppf(float x) { return __uint_as_float(__builtin_amdgcn_mov_dpp(__float_as_uint(x), CTRL, 0xf, 0xf, true)); }
DI float gsum(float x) {
  auto a = __builtin_amdgcn_permlane16_swap(__float_as_uint(x), __float_as_uint(x), false, false);
  x = __uint_as_float(a[0]) + __uint_as_float(a[1]);
  auto b = __builtin_amdgcn_permlane32_swap(__float_as_uint(x), __float_as_uint(x), false, false);
  return __uint_as_float(b[0]) + __uint_as_float(b[1]);
}
DI float gmax(float x) {
  auto a = __builtin_amdgcn_permlane16_swap(__float_as_uint(x), __float_as_uint(x), false, false);
  x = fmaxf(__uint_as_float(a[0]), __uint_as_float(a[1]));
  auto b = __builtin_amdgcn_permlane32_swap(__float_as_uint(x), __float_as_uint(x), false, false);
  return fmaxf(__uint_as_float(b[0]), __uint_as_float(b[1]));
}
DI v4u widen(v2u a, v2u b) {
  auto x = __builtin_amdgcn_permlane16_swap(a.x, b.x, false, false);
  auto y = __builtin_amdgcn_permlane16_swap(a.y, b.y, false, false);
  v4u r = {x[0], y[0], x[1], y[1]};
  return r;
}

#define STAMP(i)

struct Args {
  const void *x, *wqkv, *wo, *w1, *b1, *w2, *b2;
  void *out, *ws;
};

__global__ __launch_bounds__(512, 2) void fused_kernel(Args a) {
  extern __shared__ __attribute__((aligned(16))) char smem[];
  STAMP(0);
  const int tid = threadIdx.x, l = tid & 63, w = __builtin_amdgcn_readfirstlane(tid >> 6);
  const int c = l & 15, g = l >> 4, q4 = (l & 15) >> 2, p4 = l & 3;
  const int wq_ = w & 3, wh = w >> 2;
  const int b = blockIdx.x, xcd = b & 7, bi = b >> 3;
  const int R = 8 * (xcd >> 1) + (bi >> 2), C = 4 * (xcd & 1) + (bi & 3);
  const int FT = 7 * (xcd >> 1) + (bi >> 2);
  const bool p3 = (bi >> 2) < 7;
  const v4u rx = mkrs(a.x, S_ * D_ * 2), rqkv = mkrs(a.wqkv, D_ * 1536 * 2), rwo = mkrs(a.wo, D_ * D_ * 2), rw1 = mkrs(a.w1, S_ * F_ * 2),
            rw2 = mkrs(a.w2, F_ * S_ * 2), rws = mkrs(a.ws, WS_BYTES);
  const u32 tag = *(const u32*)a.ws + 0x9E3779B9u;

  const u32 vxa = (l >> 3) * 1024 + (((l & 7) ^ (l >> 3)) << 4);
  const u32 vwb = (8 * w + (l >> 3)) * 3072 + (((l & 7) ^ ((((l >> 4) & 1) << 1) | ((w & 1) << 2))) << 4);
  const int r0 = R * 64 - 16;
  const u32 xrow_a = (u32)max(r0 + 8 * w, 0) * 1024u, xrow_b = (u32)(r0 + 64 + 8 * w) * 1024u;
  const int rot1 = ((bi >> 2) + 2 * (bi & 3)) & 7;
  const u32 mw1k = w * 1024, mw3k = w * 3072;
#define stage_issue(ch)                                                              \
  {                                                                                  \
    const u32 kc = ((ch) + rot1) & 7;                                                \
    if (w < 2) dma1(rx, vxa, xrow_b + kc * 128, mw1k, ((ch)&3) * STG + 8192);        \
    dma1(rx, vxa, xrow_a + kc * 128, mw1k, ((ch)&3) * STG);                          \
    dma3(rqkv, vwb, kc * (64 * 3072) + C * 128, mw3k, ((ch)&3) * STG + 10240);       \
  }
  stage_issue(0);
  {
    u64 pc;
    asm volatile("s_getpc_b64 %0" : "=s"(pc));
    const v4u rc = mkrs((const void*)(pc & ~127ull), CODE_BYTES);
    if (l == 0) {
      const u32 vo = (bi * 8 + w) * 128;
      asm volatile("s_mov_b32 m0, %2\n\ts_nop 0\n\tbuffer_load_dword %0, %1, 0 offen lds" ::"v"(vo), "s"(rc), "s"(L_MISC1 + 4096) : "memory");
    }
  }
  if (C == 0 && w == 7 && l < 32) {
    v4u z = {0, 0, 0, 0};
    st_sc1_128(rws, WS_STATS + R * 512 + l * 16, z);
  }
  stage_issue(1);
  v2u xres0, xres1, b1v;
  u32 b2v;
  {
    const u32 vo = (R * 64 + 16 * wq_ + c) * 1024 + (64 * C + 32 * wh + 4 * g) * 2;
    asm volatile("buffer_load_dwordx2 %0, %2, %3, 0 offen\n\tbuffer_load_dwordx2 %1, %2, %3, 0 offen offset:32" : "=&v"(xres0), "=&v"(xres1) : "v"(vo), "s"(rx) : "memory");
    const v4u rb1 = mkrs(a.b1, F_ * 2), rb2 = mkrs(a.b2, S_ * 2);
    const u32 vo1 = (FT * 64 + 16 * wq_ + 4 * g) * 2, vo2 = (R * 64 + 16 * wq_ + c) * 2;
    asm volatile("buffer_load_dwordx2 %0, %1, %2, 0 offen" : "=&v"(b1v) : "v"(vo1), "s"(rb1) : "memory");
    asm volatile("buffer_load_ushort %0, %1, %2, 0 offen" : "=&v"(b2v) : "v"(vo2), "s"(rb2) : "memory");
  }
  stage_issue(2);
  stage_issue(3);

  const u32 vsw = ((l & 7) ^ (((l >> 4) & 1) << 1)) << 4;
  auto wvoff_a = [&](u32 RS) { return (l >> 3) * RS + vsw; };
  auto wvoff_b = [&](u32 RS) { return (l >> 3) * RS + (vsw ^ 64u) + 8 * RS - 1024; };
  const u32 fl = ((q4 >> 1) << 1) | ((g & 1) << 2);
  auto troff = [&](u32 t) { return (8 * g + q4) * 128 + (((2 * t) ^ fl) + (p4 >> 1)) * 16 + 8 * (p4 & 1); };
  const int rot3 = (4 * (bi & 3) + 2 * (bi >> 2) + (xcd & 1)) & 15;
  const u32 mslot = wq_ * 4096 + wh * 2048, mw4k = w * 4096;
#define w1_issue(j, slot) dma2(rw1, wvoff_a(3584), wvoff_b(3584), ((16 * wq_ + (((j) + rot3) & 15)) * 32 + 16 * wh) * 3584 + FT * 128, mslot, (slot)*16384)
  const int rot4 = (4 * (bi & 3) + 2 * (bi >> 2) + (xcd & 1)) % 14;
  auto ks4 = [&](int j) { int t = j + rot4; return (u32)(14 * wq_ + (t >= 14 ? t - 14 : t)); };
#define w2_issue(j, slot) dma2(rw2, wvoff_a(4096), wvoff_b(4096), (ks4(j) * 32 + 16 * wh) * 4096 + R * 128, mslot, (slot)*16384)
#define wo_issue(half)                                                                                                  \
  {                                                                                                                     \
    dma2(rwo, wvoff_a(1024), wvoff_b(1024), ((8 * (half) + w) * 32) * 1024 + C * 128, mw4k, (half)*32768);              \
    dma2(rwo, wvoff_a(1024), wvoff_b(1024), ((8 * (half) + w) * 32 + 16) * 1024 + C * 128, mw4k, (half)*32768 + 2048);  \
  }

  f4 aQ0 = {0, 0, 0, 0}, aQ1 = aQ0, aQ2 = aQ0, aK0 = aQ0, aK1 = aQ0, aK2 = aQ0, aV0 = aQ0, aV1 = aQ0, aV2 = aQ0;
  float s1a = 0.f, s2a = 0.f, s1b = 0.f, s2b = 0.f;
  const u32 ONES = 0x3F803F80u;
  const u32 xoff = (2 * wh) * 2048 + c * 128 + ((g ^ (c & 7)) << 4);
  const u32 woff = 10240 + g * 3072 + q4 * 128 + (((2 * wq_) ^ fl) + (p4 >> 1)) * 16 + 8 * (p4 & 1);
#pragma unroll
  for (int ch = 0; ch < 8; ++ch) {
    if (ch == 0) WAITV(12); else WAITV(8);
    BAR();
    if (ch == 0) STAMP(1);
    if (ch >= 1 && ch <= 4) stage_issue(ch + 3);
    if (ch == 5) wo_issue(0);
    if (ch == 6) wo_issue(1);
    if (ch == 7 && p3) { w1_issue(0, 4); w1_issue(1, 5); }
    const u32 base = (ch & 3) * STG;
    {
      v4u v = lds128(base + w * 1024 + l * 16);
      s1a = dot2(v.x, ONES, s1a); s2a = dot2(v.x, v.x, s2a);
      s1a = dot2(v.y, ONES, s1a); s2a = dot2(v.y, v.y, s2a);
      s1a = dot2(v.z, ONES, s1a); s2a = dot2(v.z, v.z, s2a);
      s1a = dot2(v.w, ONES, s1a); s2a = dot2(v.w, v.w, s2a);
      if (w < 2) {
        v = lds128(base + (8 + w) * 1024 + l * 16);
        s1b = dot2(v.x, ONES, s1b); s2b = dot2(v.x, v.x, s2b);
        s1b = dot2(v.y, ONES, s1b); s2b = dot2(v.y, v.y, s2b);
        s1b = dot2(v.z, ONES, s1b); s2b = dot2(v.z, v.z, s2b);
        s1b = dot2(v.w, ONES, s1b); s2b = dot2(v.w, v.w, s2b);
      }
      if (ch == 7) {
        auto fin = [&](float s1, float s2, int row) {
          s1 += dppf<0xB1>(s1); s2 += dppf<0xB1>(s2);
          s1 += dppf<0x4E>(s1); s2 += dppf<0x4E>(s2);
          s1 += dppf<0x141>(s1); s2 += dppf<0x141>(s2);
          const float m = s1 * (1.f / 512.f);
          const float var = fmaxf(s2 * (1.f / 512.f) - m * m, 0.f);
          const float r = rsqrtf(var + 1e-5f);
          f2 o = {r, r * m};
          if ((l & 7) == 0) *(LAS f2*)(L_MISC1 + row * 8) = o;
        };
        fin(s1a, s2a, 8 * w + (l >> 3));
        if (w < 2) fin(s1b, s2b, 64 + 8 * w + (l >> 3));
      }
    }
#pragma unroll
    for (int k2 = 0; k2 < 2; ++k2) {
      const u32 xa = base + (xoff ^ (k2 * 64)), wa = base + woff + k2 * 4 * 3072;
      v4u x0 = lds128(xa), x1 = lds128(xa + 2048), x2 = lds128(xa + 4096);
      v4u fq = trfrag(wa), fk = trfrag(wa + 1024), fv = trfrag(wa + 2048);
      if (c == 0) x0 = v4u{ONES, ONES, ONES, ONES};
      aQ1 = mfma32(fq, x1, aQ1);
      aQ2 = mfma32(fq, x2, aQ2);
      aK1 = mfma32(fk, x1, aK1);
      aK2 = mfma32(fk, x2, aK2);
      aV1 = mfma32(x1, fv, aV1);
      aV2 = mfma32(x2, fv, aV2);
      aQ0 = mfma32(fq, x0, aQ0);
      aK0 = mfma32(fk, x0, aK0);
      aV0 = mfma32(x0, fv, aV0);
    }
  }
  BAR();
  STAMP(2);
  {
    const u32 tb = L_MISC1 + (2 * wh) * 128;
    const f2 t0 = *(LAS f2*)(tb + c * 8), t1 = *(LAS f2*)(tb + 128 + c * 8), t2 = *(LAS f2*)(tb + 256 + c * 8);
    f4 csq, csk;
#pragma unroll
    for (int i = 0; i < 4; ++i) {
      csq[i] = dppf<0x150>(aQ0[i]);
      csk[i] = dppf<0x150>(aK0[i]);
    }
    const float csv = __shfl(aV0[0], c);
    auto foldT = [&](f4 acc, f2 t, f4 cs) {
      v2u r;
      r.x = pk(acc[0] * t.x - t.y * cs[0], acc[1] * t.x - t.y * cs[1]);
      r.y = pk(acc[2] * t.x - t.y * cs[2], acc[3] * t.x - t.y * cs[3]);
      return r;
    };
    auto foldV = [&](f4 acc, int j) {
      const f4 u0 = lds128f(tb + j * 128 + g * 32), u1 = lds128f(tb + j * 128 + g * 32 + 16);
      v2u r;
      r.x = pk(acc[0] * u0[0] - u0[1] * csv, acc[1] * u0[2] - u0[3] * csv);
      r.y = pk(acc[2] * u1[0] - u1[1] * csv, acc[3] * u1[2] - u1[3] * csv);
      return r;
    };
    const v2u Q1 = foldT(aQ1, t1, csq), Q2 = foldT(aQ2, t2, csq);
    const v2u K0 = foldT(aK0, t0, csk), K1 = foldT(aK1, t1, csk), K2 = foldT(aK2, t2, csk);
    const v2u V0 = foldV(aV0, 0), V1 = foldV(aV1, 1), V2 = foldV(aV2, 2);
    const bool odd = c & 1;
    const bool cur0 = (4 * g + (c & 1)) <= c, cur1 = (4 * g + 2 + (c & 1)) <= c;
    const f4 z4 = {0, 0, 0, 0};
    auto attend = [&](v2u Q, v2u Kp, v2u Kc, v2u Vp, v2u Vc, bool noprev) {
      const f4 sp = mfma16(Kp, Q, z4), sc = mfma16(Kc, Q, z4);
      const float p0v = odd ? sp[1] : sp[0], p1v = odd ? sp[3] : sp[2];
      const float c0v = odd ? sc[1] : sc[0], c1v = odd ? sc[3] : sc[2];
      float v0 = cur0 ? c0v : (noprev ? -3.0e38f : p0v), v1 = cur1 ? c1v : (noprev ? -3.0e38f : p1v);
      const float mx = gmax(fmaxf(v0, v1));
      const float kk = 0.25f * 1.44269504088896f;
      const float e0 = __builtin_amdgcn_exp2f((v0 - mx) * kk), e1 = __builtin_amdgcn_exp2f((v1 - mx) * kk);
      const u32 pp = pk(e0, e1);
      const float sum = gsum(blo(pp) + bhi(pp));
      const u32 w0 = odd ? (pp << 16) : (pp & 0xffffu), w1 = odd ? (pp & 0xffff0000u) : (pp >> 16);
      const v4u P = {cur0 ? 0u : w0, cur1 ? 0u : w1, cur0 ? w0 : 0u, cur1 ? w1 : 0u};
      const v4u V = {Vp.x, Vp.y, Vc.x, Vc.y};
      const f4 o = mfma32(V, P, z4);
      const float inv = __builtin_amdgcn_rcpf(sum);
      v2u r;
      r.x = pk(o[0] * inv, o[1] * inv);
      r.y = pk(o[2] * inv, o[3] * inv);
      return r;
    };
    const v2u oa = attend(Q1, K0, K1, V0, V1, R == 0 && wh == 0), ob = attend(Q2, K1, K2, V1, V2, false);
    const v4u ov = widen(oa, ob);
    const u32 tt = 4 * R + 2 * wh + (g & 1), ks = 2 * C + (wq_ >> 1);
    st_sc1_128(rws, WS_OF + (tt * 16 + ks) * 1024 + (c + 16 * ((wq_ & 1) * 2 + (g >> 1))) * 16, ov);
  }
  STAMP(3);
  if (p3) {
    w1_issue(2, 6); w1_issue(3, 7); w1_issue(4, 8);
    WAITV(6);
  } else {
    WAITV(0);
  }
  asm volatile("" : "+v"(xres0), "+v"(xres1), "+v"(b1v), "+v"(b2v));
  BAR();
  if (w == 0 && l < 8) st_sc1_32(rws, WS_FLAGA + (R * 8 + l) * 128 + C * 4, tag);
  STAMP(4);
  v2u hp0, hp1;
  {
    v4u wf[8][4];
#pragma unroll
    for (int kk = 0; kk < 8; ++kk)
#pragma unroll
      for (int t = 0; t < 4; ++t) wf[kk][t] = trfrag((8 * wh + kk) * 4096 + troff(t ^ (2 * wh)));
    BAR();
    if (p3) { w1_issue(5, 0); w1_issue(6, 1); }
    if (w == 1) {
      const u32 vo = WS_FLAGA + (R * 8 + C) * 128 + (l & 7) * 4;
      for (u32 sp = 0; sp < SPIN_CAP; ++sp) {
        u32 f;
        asm volatile("buffer_load_dword %0, %1, %2, 0 offen sc1\n\ts_waitcnt vmcnt(0)" : "=v"(f) : "v"(vo), "s"(rws) : "memory");
        if (__all(f == tag)) break;
      }
    }
    BAR();
    STAMP(5);
    v4u of[8];
    {
      const u32 vo = l * 16, so = WS_OF + ((4 * R + wq_) * 16 + 8 * wh) * 1024;
      LD_SC1(of[0], rws, vo, so, 0); LD_SC1(of[1], rws, vo, so, 1024); LD_SC1(of[2], rws, vo, so, 2048); LD_SC1(of[3], rws, vo, so, 3072);
      const u32 so2 = so + 4096;
      LD_SC1(of[4], rws, vo, so2, 0); LD_SC1(of[5], rws, vo, so2, 1024); LD_SC1(of[6], rws, vo, so2, 2048); LD_SC1(of[7], rws, vo, so2, 3072);
    }
    if (p3) { w1_issue(7, 2); w1_issue(8, 3); } else { w2_issue(0, 0); w2_issue(1, 1); }
    f4 acc[4] = {{0, 0, 0, 0}, {0, 0, 0, 0}, {0, 0, 0, 0}, {0, 0, 0, 0}};
#define P2STEP(kk) WAITV1(4 + 7 - kk, of[kk]); _Pragma("unroll") for (int t = 0; t < 4; ++t) acc[t] = mfma32(wf[kk][t], of[kk], acc[t]);
    P2STEP(0) P2STEP(1) P2STEP(2) P2STEP(3) P2STEP(4) P2STEP(5) P2STEP(6) P2STEP(7)
    STAMP(6);
    *(LAS f4*)(L_M + w * 2048 + l * 16) = acc[2];
    *(LAS f4*)(L_M + w * 2048 + 1024 + l * 16) = acc[3];
    BAR();
    const f4 h0 = acc[0] + lds128f(L_M + (w ^ 4) * 2048 + l * 16), h1 = acc[1] + lds128f(L_M + (w ^ 4) * 2048 + 1024 + l * 16);
    hp0.x = pk(h0[0] + blo(xres0.x), h0[1] + bhi(xres0.x));
    hp0.y = pk(h0[2] + blo(xres0.y), h0[3] + bhi(xres0.y));
    hp1.x = pk(h1[0] + blo(xres1.x), h1[1] + bhi(xres1.x));
    hp1.y = pk(h1[2] + blo(xres1.y), h1[3] + bhi(xres1.y));
    float s1 = dot2(hp0.x, ONES, 0.f), s2 = dot2(hp0.x, hp0.x, 0.f);
    s1 = dot2(hp0.y, ONES, s1); s2 = dot2(hp0.y, hp0.y, s2);
    s1 = dot2(hp1.x, ONES, s1); s2 = dot2(hp1.x, hp1.x, s2);
    s1 = dot2(hp1.y, ONES, s1); s2 = dot2(hp1.y, hp1.y, s2);
    s1 = gsum(s1); s2 = gsum(s2);
    if (g < 2) {
      const u32 vo = WS_STATS + (R * 64 + 16 * wq_ + c) * 8 + g * 4;
      const float v = g ? s2 : s1;
      asm volatile("buffer_atomic_add_f32 %0, %1, %2, 0 offen sc1" ::"v"(v), "v"(vo), "s"(rws) : "memory");
    }
    const u32 tb = L_M + (w ^ 4) * 2048;
    asm volatile("s_waitcnt lgkmcnt(0)" ::: "memory");
    *(LAS v2u*)(tb + c * 64 + g * 8) = hp0;
    *(LAS v2u*)(tb + c * 64 + 32 + g * 8) = hp1;
    asm volatile("s_waitcnt lgkmcnt(0)" ::: "memory");
    const u32 ta = tb + (8 * (g >> 1) + q4) * 64 + (g & 1) * 32 + p4 * 8;
    const v2u y0 = trd(ta), y1 = trd(ta + 256);
    const v4u hv = {y0.x, y0.y, y1.x, y1.y};
    const u32 dtile = 4 * C + 2 * wh + (g & 1), ks = 2 * R + (wq_ >> 1);
    st_sc1_128(rws, WS_HF + (dtile * 64 + ks) * 1024 + (c + 16 * ((wq_ & 1) * 2 + (g >> 1))) * 16, hv);
  }
  STAMP(7);
  v4u bw[7][4];
  u32 tro[4];
#pragma unroll
  for (int t = 0; t < 4; ++t) tro[t] = wq_ * 4096 + troff(t ^ wq_);
  f4 acc[4][2];
  if (p3) {
#pragma unroll
    for (int j = 0; j < 7; ++j)
#pragma unroll
      for (int t = 0; t < 4; ++t) bw[j][t] = trfrag(((j < 5) ? (4 + j) : (j - 5)) * 16384 + tro[t]);
    WAITV(0);
    BAR();
    if (w == 0 && l < REPC) st_sc1_32(rws, WS_FLAGC + l * 1024 + b * 4, tag);
    STAMP(8);
#pragma unroll
    for (int j = 9; j < 16; ++j) w1_issue(j, (j < 14) ? (j - 5) : (j - 14));
    if (w == 1) {
      const u32 vo = WS_FLAGC + (b % REPC) * 1024 + l * 16;
      for (u32 sp = 0; sp < SPIN_CAP; ++sp) {
        v4u f;
        asm volatile("buffer_load_dwordx4 %0, %1, %2, 0 offen sc1\n\ts_waitcnt vmcnt(0)" : "=v"(f) : "v"(vo), "s"(rws) : "memory");
        if (__all(f.x == tag && f.y == tag && f.z == tag && f.w == tag)) break;
      }
    }
    BAR();
    STAMP(9);
    if (b == 0 && tid == 0) st_sc1_32(rws, WS_EPOCH, tag);
    v4u st0, st1;
    {
      const u32 vo = tid * 32;
      LD_SC1(st0, rws, vo, WS_STATS + 0u, 0);
      LD_SC1(st1, rws, vo, WS_STATS + 0u, 16);
    }
    constexpr int PF = 5;
    v4u af[16][2];
    const u32 avo = l * 16;
    const u32 abase = WS_HF + ((4 * C + 2 * wh) * 64 + 16 * wq_) * 1024;
#define A3LOAD(n) { const u32 so = abase + (((n) + 7 + rot3) & 15) * 1024; LD_SC1(af[n][0], rws, avo, so, 0); const u32 so2 = so + 65536; LD_SC1(af[n][1], rws, avo, so2, 0); }
    A3LOAD(0) A3LOAD(1) A3LOAD(2) A3LOAD(3) A3LOAD(4)
    WAITV2(2 * PF, st0, st1);
    {
      auto fin = [&](u32 a1, u32 a2, float& r, float& cc) {
        const float m = __uint_as_float(a1) * (1.f / 512.f);
        const float var = fmaxf(__uint_as_float(a2) * (1.f / 512.f) - m * m, 0.f);
        const float r1 = rsqrtf(var + 1e-5f);
        const float r2 = rsqrtf(var * r1 * r1 + 1e-5f);
        r = r1 * r2;
        cc = -m * r;
      };
      f4 rr, cc;
      float r, c_;
      fin(st0.x, st0.y, r, c_); rr[0] = r; cc[0] = c_;
      fin(st0.z, st0.w, r, c_); rr[1] = r; cc[1] = c_;
      fin(st1.x, st1.y, r, c_); rr[2] = r; cc[2] = c_;
      fin(st1.z, st1.w, r, c_); rr[3] = r; cc[3] = c_;
      *(LAS f4*)(L_M + tid * 16) = rr;
      *(LAS f4*)(L_M + 8192 + tid * 16) = cc;
    }
    BAR();
#pragma unroll
    for (int t = 0; t < 4; ++t) acc[t][0] = acc[t][1] = f4{0, 0, 0, 0};
    auto norm = [&](v4u v, f4 r0v, f4 r1v, f4 c0v, f4 c1v) {
      v4u o;
      o.x = pk(blo(v.x) * r0v[0] + c0v[0], bhi(v.x) * r0v[1] + c0v[1]);
      o.y = pk(blo(v.y) * r0v[2] + c0v[2], bhi(v.y) * r0v[3] + c0v[3]);
      o.z = pk(blo(v.z) * r1v[0] + c1v[0], bhi(v.z) * r1v[1] + c1v[1]);
      o.w = pk(blo(v.w) * r1v[2] + c1v[2], bhi(v.w) * r1v[3] + c1v[3]);
      return o;
    };
    const u32 tbase = L_M + (16 * wq_ * 32 + 8 * g) * 4;
#define P3STEP(n, NW, BF)                                                                        \
  {                                                                                              \
    const u32 ta = tbase + (((n) + 7 + rot3) & 15) * 128;                                        \
    const f4 r0v = lds128f(ta), r1v = lds128f(ta + 16), c0v = lds128f(ta + 8192), c1v = lds128f(ta + 8192 + 16); \
    WAITV2(NW, af[n][0], af[n][1]);                                                              \
    const v4u h0 = norm(af[n][0], r0v, r1v, c0v, c1v), h1 = norm(af[n][1], r0v, r1v, c0v, c1v);  \
    _Pragma("unroll") for (int t = 0; t < 4; ++t) {                                              \
      const v4u bf = BF;                                                                         \
      acc[t][0] = mfma32(bf, h0, acc[t][0]);                                                     \
      acc[t][1] = mfma32(bf, h1, acc[t][1]);                                                     \
    }                                                                                            \
  }
#define SL3(n) (((n) < 7) ? ((n) + 2) : ((n)-7))
    P3STEP(0, 2 * (PF - 1), trfrag(SL3(0) * 16384 + tro[t])) A3LOAD(5)
    P3STEP(1, 2 * (PF - 1), trfrag(SL3(1) * 16384 + tro[t])) A3LOAD(6)
    P3STEP(2, 2 * (PF - 1), trfrag(SL3(2) * 16384 + tro[t])) A3LOAD(7)
    P3STEP(3, 2 * (PF - 1), trfrag(SL3(3) * 16384 + tro[t])) A3LOAD(8)
    P3STEP(4, 2 * (PF - 1), trfrag(SL3(4) * 16384 + tro[t])) A3LOAD(9)
    P3STEP(5, 2 * (PF - 1), trfrag(SL3(5) * 16384 + tro[t])) A3LOAD(10)
    P3STEP(6, 2 * (PF - 1), trfrag(SL3(6) * 16384 + tro[t])) A3LOAD(11)
    P3STEP(7, 2 * (PF - 1), trfrag(SL3(7) * 16384 + tro[t])) A3LOAD(12)
    P3STEP(8, 2 * (PF - 1), trfrag(SL3(8) * 16384 + tro[t])) A3LOAD(13) A3LOAD(14) A3LOAD(15)
    BAR();
#pragma unroll
    for (int j = 0; j < 6; ++j) w2_issue(j, j);
    P3STEP(9, 12 + 12, bw[0][t])
    P3STEP(10, 12 + 10, bw[1][t])
    P3STEP(11, 12 + 8, bw[2][t])
    P3STEP(12, 12 + 6, bw[3][t])
    P3STEP(13, 12 + 4, bw[4][t])
    P3STEP(14, 12 + 2, bw[5][t])
    P3STEP(15, 12 + 0, bw[6][t])
    STAMP(10);
    const u32 rb = 6 * 16384;
#pragma unroll
    for (int t = 1; t < 4; ++t)
#pragma unroll
      for (int m = 0; m < 2; ++m) *(LAS f4*)(rb + (w * 6 + (t - 1) * 2 + m) * 1024 + l * 16) = acc[t][m];
    BAR();
    f4 u0 = acc[0][0], u1 = acc[0][1];
#pragma unroll
    for (int t = 1; t < 4; ++t) {
      u0 += lds128f(rb + ((w ^ t) * 6 + (t - 1) * 2) * 1024 + l * 16);
      u1 += lds128f(rb + ((w ^ t) * 6 + (t - 1) * 2 + 1) * 1024 + l * 16);
    }
    const f4 bb = {blo(b1v.x), bhi(b1v.x), blo(b1v.y), bhi(b1v.y)};
    auto gelu = [&](float v) {
      const float z = v * (1.f + 0.044715f * v * v) * (-2.f * 0.7978845608028654f * 1.44269504088896f);
      return v * __builtin_amdgcn_rcpf(1.f + __builtin_amdgcn_exp2f(z));
    };
    u0 += bb;
    u1 += bb;
    v2u ua, ub;
    ua.x = pk(gelu(u0[0]), gelu(u0[1])); ua.y = pk(gelu(u0[2]), gelu(u0[3]));
    ub.x = pk(gelu(u1[0]), gelu(u1[1])); ub.y = pk(gelu(u1[2]), gelu(u1[3]));
    const v4u uv = widen(ua, ub);
    const u32 dtile = 4 * C + 2 * wh + (g & 1), ks = 2 * FT + (wq_ >> 1);
    st_sc1_128(rws, WS_UF + (dtile * 56 + ks) * 1024 + (c + 16 * ((wq_ & 1) * 2 + (g >> 1))) * 16, uv);
    STAMP(11);
    WAITV(0);
    BAR();
    if (w == 0 && l < 32) st_sc1_32(rws, WS_FLAGD + (l * 8 + C) * 128 + FT * 4, tag);
  } else {
    WAITV(0);
    BAR();
    if (w == 0 && l < REPC) st_sc1_32(rws, WS_FLAGC + l * 1024 + b * 4, tag);
#pragma unroll
    for (int j = 2; j < 6; ++j) w2_issue(j, j);
    WAITV(0);
    BAR();
  }
  STAMP(12);
#pragma unroll
  for (int j = 0; j < 5; ++j)
#pragma unroll
    for (int t = 0; t < 4; ++t) bw[j][t] = trfrag(j * 16384 + tro[t]);
  BAR();
#pragma unroll
  for (int j = 6; j < 14; ++j) w2_issue(j, (j < 9) ? j : (j - 9));
  if (w == 1) {
    const u32 vo = WS_FLAGD + (R * 8 + C) * 128 + (l < 28 ? l : 0) * 4;
    for (u32 sp = 0; sp < SPIN_CAP; ++sp) {
      u32 f;
      asm volatile("buffer_load_dword %0, %1, %2, 0 offen sc1\n\ts_waitcnt vmcnt(0)" : "=v"(f) : "v"(vo), "s"(rws) : "memory");
      if (__all(f == tag)) break;
    }
  }
  BAR();
  STAMP(13);
  {
    v4u af[14][2];
    const u32 avo = l * 16;
    const u32 abase = WS_UF + ((4 * C + 2 * wh) * 56) * 1024;
#define A4LOAD(n) { const u32 so = abase + ks4(n) * 1024; LD_SC1(af[n][0], rws, avo, so, 0); const u32 so2 = so + 56 * 1024; LD_SC1(af[n][1], rws, avo, so2, 0); }
    A4LOAD(0) A4LOAD(1) A4LOAD(2) A4LOAD(3) A4LOAD(4) A4LOAD(5) A4LOAD(6) A4LOAD(7)
#pragma unroll
    for (int t = 0; t < 4; ++t) acc[t][0] = acc[t][1] = f4{0, 0, 0, 0};
#define P4STEP(n, NW, BF)                              \
  {                                                    \
    WAITV2(NW, af[n][0], af[n][1]);                    \
    _Pragma("unroll") for (int t = 0; t < 4; ++t) {    \
      const v4u bf = BF;                               \
      acc[t][0] = mfma32(af[n][0], bf, acc[t][0]);     \
      acc[t][1] = mfma32(af[n][1], bf, acc[t][1]);     \
    }                                                  \
  }
#define SL4(n) (((n) < 9) ? (n) : ((n)-9))
    P4STEP(0, 14, bw[0][t]) A4LOAD(8)
    BAR();
    P4STEP(1, 14, bw[1][t]) A4LOAD(9)
    P4STEP(2, 14, bw[2][t]) A4LOAD(10)
    P4STEP(3, 14, bw[3][t]) A4LOAD(11)
    P4STEP(4, 14, bw[4][t]) A4LOAD(12)
    P4STEP(5, 14, trfrag(SL4(5) * 16384 + tro[t])) A4LOAD(13)
    P4STEP(6, 14, trfrag(SL4(6) * 16384 + tro[t]))
    P4STEP(7, 12, trfrag(SL4(7) * 16384 + tro[t]))
    P4STEP(8, 10, trfrag(SL4(8) * 16384 + tro[t]))
    P4STEP(9, 8, trfrag(SL4(9) * 16384 + tro[t]))
    P4STEP(10, 6, trfrag(SL4(10) * 16384 + tro[t]))
    P4STEP(11, 4, trfrag(SL4(11) * 16384 + tro[t]))
    P4STEP(12, 2, trfrag(SL4(12) * 16384 + tro[t]))
    P4STEP(13, 0, trfrag(SL4(13) * 16384 + tro[t]))
    STAMP(14);
    BAR();
#pragma unroll
    for (int t = 1; t < 4; ++t)
#pragma unroll
      for (int m = 0; m < 2; ++m) *(LAS f4*)((w * 6 + (t - 1) * 2 + m) * 1024 + l * 16) = acc[t][m];
    BAR();
    f4 y0 = acc[0][0], y1 = acc[0][1];
#pragma unroll
    for (int t = 1; t < 4; ++t) {
      y0 += lds128f(((w ^ t) * 6 + (t - 1) * 2) * 1024 + l * 16);
      y1 += lds128f(((w ^ t) * 6 + (t - 1) * 2 + 1) * 1024 + l * 16);
    }
    const float bs = blo(b2v);
    v2u oa, ob;
    oa.x = pk(y0[0] + bs + blo(hp0.x), y0[1] + bs + bhi(hp0.x));
    oa.y = pk(y0[2] + bs + blo(hp0.y), y0[3] + bs + bhi(hp0.y));
    ob.x = pk(y1[0] + bs + blo(hp1.x), y1[1] + bs + bhi(hp1.x));
    ob.y = pk(y1[2] + bs + blo(hp1.y), y1[3] + bs + bhi(hp1.y));
    const v4u ov = widen(oa, ob);
    *(v4u*)((char*)a.out + (size_t)(R * 64 + 16 * wq_ + c) * 1024 + (64 * C + 32 * wh + 16 * (g & 1) + 8 * (g >> 1)) * 2) = ov;
  }
}

extern "C" void kernel_launch(void* const* d_in, const int* in_sizes, int n_in, void* d_out, int out_size, void* d_ws, size_t ws_size, hipStream_t stream) {
  Args a;
  a.x = d_in[0];
  a.wqkv = d_in[1];
  a.wo = d_in[2];
  a.w1 = d_in[4];
  a.b1 = d_in[5];
  a.w2 = d_in[6];
  a.b2 = d_in[7];
  a.out = d_out;
  a.ws = d_ws;
  (void)hipFuncSetAttribute((const void*)fused_kernel, hipFuncAttributeMaxDynamicSharedMemorySize, LDS_BYTES);
  fused_kernel<<<256, 512, LDS_BYTES, stream>>>(a);
}
```

```cpp
#define STAMPS 1
#define ENC 0
#include <hip/hip_runtime.h>
#include <hip/hip_bf16.h>

typedef unsigned u32;
typedef unsigned long long u64;
typedef u32 v4u __attribute__((ext_vector_type(4)));
typedef u32 v2u __attribute__((ext_vector_type(2)));
typedef float f4 __attribute__((ext_vector_type(4)));
typedef float f2 __attribute__((ext_vector_type(2)));
typedef __bf16 v8bf __attribute__((ext_vector_type(8)));
typedef __bf16 v2bf __attribute__((ext_vector_type(2)));
typedef short v4s __attribute__((ext_vector_type(4)));
#define LAS __attribute__((address_space(3)))
#define DI __device__ __forceinline__

constexpr int S_ = 2048, D_ = 512, F_ = 1792;
constexpr u32 WS_EPOCH = 0, WS_FLAGA = 4096, WS_FLAGD = WS_FLAGA + 32768, WS_FLAGC = WS_FLAGD + 32768, WS_STATS = WS_FLAGC + 32768;
constexpr u32 WS_STAMP = 512 * 1024;
constexpr u32 WS_OF = 1u << 20, WS_HF = 3u << 20, WS_UF = 5u << 20, WS_BYTES = 8u << 20;
constexpr int REPC = 16;
constexpr u32 STG = 34816, L_MISC1 = 152 * 1024, L_M = 144 * 1024, LDS_BYTES = 160 * 1024;
constexpr u32 SPIN_CAP = 1u << 16;
constexpr u32 CODE_BYTES = 28000;

DI v4u mkrs(const void* p, u32 bytes) {
  v4u r;
  r.x = (u32)(size_t)p;
  r.y = (u32)((size_t)p >> 32) & 0xffffu;
  r.z = bytes;
  r.w = 0x00020000u;
  return r;
}
#define dma1(rs, voff, soff, m0b, imm) \
  asm volatile("s_add_u32 m0, %3, %4\n\ts_nop 0\n\tbuffer_load_dwordx4 %0, %1, %2 offen lds" ::"v"(voff), "s"(rs), "s"(soff), "s"(m0b), "i"(imm) : "memory", "scc")
#define dma2(rs, voffa, voffb, soff, m0b, imm)                                                                                                        \
  asm volatile("s_add_u32 m0, %4, %5\n\ts_nop 0\n\tbuffer_load_dwordx4 %0, %2, %3 offen lds\n\tbuffer_load_dwordx4 %1, %2, %3 offen offset:1024 lds" ::"v"(voffa), \
               "v"(voffb), "s"(rs), "s"(soff), "s"(m0b), "i"(imm)                                                                                     \
               : "memory", "scc")
#define dma3(rs, voff, soff, m0b, imm)                                                                                                               \
  asm volatile(                                                                                                                                      \
      "s_add_u32 m0, %3, %4\n\ts_nop 0\n\tbuffer_load_dwordx4 %0, %1, %2 offen lds\n\tbuffer_load_dwordx4 %0, %1, %2 offen offset:1024 lds\n\tbuffer_load_dwordx4 %0, %1, %2 offen offset:2048 lds" ::"v"(voff), \
      "s"(rs), "s"(soff), "s"(m0b), "i"(imm)                                                                                                         \
      : "memory", "scc")
#define LD_SC1(dst, rs, voff, soff, imm) asm volatile("buffer_load_dwordx4 %0, %1, %2, %3 offen offset:%4 sc1" : "=v"(dst) : "v"(voff), "s"(rs), "s"(soff), "i"(imm) : "memory")
#define WAITV(n) asm volatile("s_waitcnt vmcnt(%0)" ::"i"(n) : "memory")
#define WAITV1(n, a) asm volatile("s_waitcnt vmcnt(%1)" : "+v"(a) : "i"(n) : "memory")
#define WAITV2(n, a, b) asm volatile("s_waitcnt vmcnt(%2)" : "+v"(a), "+v"(b) : "i"(n) : "memory")
#define BAR() asm volatile("s_waitcnt lgkmcnt(0)\n\ts_barrier" ::: "memory")
DI void st_sc1_128(v4u rs, u32 voff, v4u v) { asm volatile("buffer_store_dwordx4 %0, %1, %2, 0 offen sc1\n\ts_nop 1" ::"v"(v), "v"(voff), "s"(rs) : "memory"); }
DI void st_sc1_32(v4u rs, u32 voff, u32 v) { asm volatile("buffer_store_dword %0, %1, %2, 0 offen sc1" ::"v"(v), "v"(voff), "s"(rs) : "memory"); }

DI f4 mfma32(v4u a, v4u b, f4 c) { return __builtin_amdgcn_mfma_f32_16x16x32_bf16(__builtin_bit_cast(v8bf, a), __builtin_bit_cast(v8bf, b), c, 0, 0, 0); }
DI f4 mfma16(v2u a, v2u b, f4 c) { return __builtin_amdgcn_mfma_f32_16x16x16bf16_1k(__builtin_bit_cast(v4s, a), __builtin_bit_cast(v4s, b), c, 0, 0, 0); }
DI u32 pk(float a, float b) {
  f2 v = {a, b};
  return __builtin_bit_cast(u32, __builtin_convertvector(v, v2bf));
}
DI float blo(u32 v) { return __uint_as_float(v << 16); }
DI float bhi(u32 v) { return __uint_as_float(v & 0xffff0000u); }
DI float dot2(u32 a, u32 b, float c) { return __builtin_amdgcn_fdot2_f32_bf16(__builtin_bit_cast(v2bf, a), __builtin_bit_cast(v2bf, b), c, false); }
DI v4u lds128(u32 a) { return *(LAS v4u*)a; }
DI f4 lds128f(u32 a) { return *(LAS f4*)a; }
DI v2u trd(u32 a) { return __builtin_bit_cast(v2u, __builtin_amdgcn_ds_read_tr16_b64_v4i16((LAS v4s*)a)); }
DI v4u trfrag(u32 a) {
  v2u x = trd(a), y = trd(a + 512);
  v4u r = {x.x, x.y, y.x, y.y};
  return r;
}
template <int CTRL> DI float dppf(float x) { return __uint_as_float(__builtin_amdgcn_mov_dpp(__float_as_uint(x), CTRL, 0xf, 0xf, true)); }
DI float gsum(float x) {
  auto a = __builtin_amdgcn_permlane16_swap(__float_as_uint(x), __float_as_uint(x), false, false);
  x = __uint_as_float(a[0]) + __uint_as_float(a[1]);
  auto b = __builtin_amdgcn_permlane32_swap(__float_as_uint(x), __float_as_uint(x), false, false);
  return __uint_as_float(b[0]) + __uint_as_float(b[1]);
}
DI float gmax(float x) {
  auto a = __builtin_amdgcn_permlane16_swap(__float_as_uint(x), __float_as_uint(x), false, false);
  x = fmaxf(__uint_as_float(a[0]), __uint_as_float(a[1]));
  auto b = __builtin_amdgcn_permlane32_swap(__float_as_uint(x), __float_as_uint(x), false, false);
  return fmaxf(__uint_as_float(b[0]), __uint_as_float(b[1]));
}
DI v4u widen(v2u a, v2u b) {
  auto x = __builtin_amdgcn_permlane16_swap(a.x, b.x, false, false);
  auto y = __builtin_amdgcn_permlane16_swap(a.y, b.y, false, false);
  v4u r = {x[0], y[0], x[1], y[1]};
  return r;
}

#define STAMP(i) do { asm volatile("s_memrealtime %0\n\ts_waitcnt lgkmcnt(0)" : "=s"(stp) :: "memory"); stamp[i] = (u32)stp; } while (0)

struct Args {
  const void *x, *wqkv, *wo, *w1, *b1, *w2, *b2;
  void *out, *ws;
};

__global__ __launch_bounds__(512, 2) void fused_kernel(Args a) {
  extern __shared__ __attribute__((aligned(16))) char smem[];
  u64 stp;
  u32 stamp[16];
  STAMP(0);
  const int tid = threadIdx.x, l = tid & 63, w = __builtin_amdgcn_readfirstlane(tid >> 6);
  const int c = l & 15, g = l >> 4, q4 = (l & 15) >> 2, p4 = l & 3;
  const int wq_ = w & 3, wh = w >> 2;
  const int b = blockIdx.x, xcd = b & 7, bi = b >> 3;
  const int R = 8 * (xcd >> 1) + (bi >> 2), C = 4 * (xcd & 1) + (bi & 3);
  const int FT = 7 * (xcd >> 1) + (bi >> 2);
  const bool p3 = (bi >> 2) < 7;
  const v4u rx = mkrs(a.x, S_ * D_ * 2), rqkv = mkrs(a.wqkv, D_ * 1536 * 2), rwo = mkrs(a.wo, D_ * D_ * 2), rw1 = mkrs(a.w1, S_ * F_ * 2),
            rw2 = mkrs(a.w2, F_ * S_ * 2), rws = mkrs(a.ws, WS_BYTES);
  const u32 tag = *(const u32*)a.ws + 0x9E3779B9u;

  const u32 vxa = (l >> 3) * 1024 + (((l & 7) ^ (l >> 3)) << 4);
  const u32 vwb = (8 * w + (l >> 3)) * 3072 + (((l & 7) ^ ((((l >> 4) & 1) << 1) | ((w & 1) << 2))) << 4);
  const int r0 = R * 64 - 16;
  const u32 xrow_a = (u32)max(r0 + 8 * w, 0) * 1024u, xrow_b = (u32)(r0 + 64 + 8 * w) * 1024u;
  const int rot1 = ((bi >> 2) + 2 * (bi & 3)) & 7;
  const u32 mw1k = w * 1024, mw3k = w * 3072;
#define stage_issue(ch)                                                              \
  {                                                                                  \
    const u32 kc = ((ch) + rot1) & 7;                                                \
    if (w < 2) dma1(rx, vxa, xrow_b + kc * 128, mw1k, ((ch)&3) * STG + 8192);        \
    dma1(rx, vxa, xrow_a + kc * 128, mw1k, ((ch)&3) * STG);                          \
    dma3(rqkv, vwb, kc * (64 * 3072) + C * 128, mw3k, ((ch)&3) * STG + 10240);       \
  }
  stage_issue(0);
  {
    u64 pc;
    asm volatile("s_getpc_b64 %0" : "=s"(pc));
    const v4u rc = mkrs((const void*)(pc & ~127ull), CODE_BYTES);
    if (l == 0) {
      const u32 vo = (bi * 8 + w) * 128;
      asm volatile("s_mov_b32 m0, %2\n\ts_nop 0\n\tbuffer_load_dword %0, %1, 0 offen lds" ::"v"(vo), "s"(rc), "s"(L_MISC1 + 4096) : "memory");
    }
  }
  if (C == 0 && w == 7 && l < 32) {
    v4u z = {0, 0, 0, 0};
    st_sc1_128(rws, WS_STATS + R * 512 + l * 16, z);
  }
  stage_issue(1);
  v2u xres0, xres1, b1v;
  u32 b2v;
  {
    const u32 vo = (R * 64 + 16 * wq_ + c) * 1024 + (64 * C + 32 * wh + 4 * g) * 2;
    asm volatile("buffer_load_dwordx2 %0, %2, %3, 0 offen\n\tbuffer_load_dwordx2 %1, %2, %3, 0 offen offset:32" : "=&v"(xres0), "=&v"(xres1) : "v"(vo), "s"(rx) : "memory");
    const v4u rb1 = mkrs(a.b1, F_ * 2), rb2 = mkrs(a.b2, S_ * 2);
    const u32 vo1 = (FT * 64 + 16 * wq_ + 4 * g) * 2, vo2 = (R * 64 + 16 * wq_ + c) * 2;
    asm volatile("buffer_load_dwordx2 %0, %1, %2, 0 offen" : "=&v"(b1v) : "v"(vo1), "s"(rb1) : "memory");
    asm volatile("buffer_load_ushort %0, %1, %2, 0 offen" : "=&v"(b2v) : "v"(vo2), "s"(rb2) : "memory");
  }
  stage_issue(2);
  stage_issue(3);

  const u32 vsw = ((l & 7) ^ (((l >> 4) & 1) << 1)) << 4;
  auto wvoff_a = [&](u32 RS) { return (l >> 3) * RS + vsw; };
  auto wvoff_b = [&](u32 RS) { return (l >> 3) * RS + (vsw ^ 64u) + 8 * RS - 1024; };
  const u32 fl = ((q4 >> 1) << 1) | ((g & 1) << 2);
  auto troff = [&](u32 t) { return (8 * g + q4) * 128 + (((2 * t) ^ fl) + (p4 >> 1)) * 16 + 8 * (p4 & 1); };
  const int rot3 = (4 * (bi & 3) + 2 * (bi >> 2) + (xcd & 1)) & 15;
  const u32 mslot = wq_ * 4096 + wh * 2048, mw4k = w * 4096;
#define w1_issue(j, slot) dma2(rw1, wvoff_a(3584), wvoff_b(3584), ((16 * wq_ + (((j) + rot3) & 15)) * 32 + 16 * wh) * 3584 + FT * 128, mslot, (slot)*16384)
  const int rot4 = (4 * (bi & 3) + 2 * (bi >> 2) + (xcd & 1)) % 14;
  auto ks4 = [&](int j) { int t = j + rot4; return (u32)(14 * wq_ + (t >= 14 ? t - 14 : t)); };
#define w2_issue(j, slot) dma2(rw2, wvoff_a(4096), wvoff_b(4096), (ks4(j) * 32 + 16 * wh) * 4096 + R * 128, mslot, (slot)*16384)
#define wo_issue(half)                                                                                                  \
  {                                                                                                                     \
    dma2(rwo, wvoff_a(1024), wvoff_b(1024), ((8 * (half) + w) * 32) * 1024 + C * 128, mw4k, (half)*32768);              \
    dma2(rwo, wvoff_a(1024), wvoff_b(1024), ((8 * (half) + w) * 32 + 16) * 1024 + C * 128, mw4k, (half)*32768 + 2048);  \
  }

  f4 aQ0 = {0, 0, 0, 0}, aQ1 = aQ0, aQ2 = aQ0, aK0 = aQ0, aK1 = aQ0, aK2 = aQ0, aV0 = aQ0, aV1 = aQ0, aV2 = aQ0;
  float s1a = 0.f, s2a = 0.f, s1b = 0.f, s2b = 0.f;
  const u32 ONES = 0x3F803F80u;
  const u32 xoff = (2 * wh) * 2048 + c * 128 + ((g ^ (c & 7)) << 4);
  const u32 woff = 10240 + g * 3072 + q4 * 128 + (((2 * wq_) ^ fl) + (p4 >> 1)) * 16 + 8 * (p4 & 1);
#pragma unroll
  for (int ch = 0; ch < 8; ++ch) {
    if (ch == 0) WAITV(12); else WAITV(8);
    BAR();
    if (ch == 0) STAMP(1);
    if (ch >= 1 && ch <= 4) stage_issue(ch + 3);
    if (ch == 5) wo_issue(0);
    if (ch == 6) wo_issue(1);
    if (ch == 7 && p3) { w1_issue(0, 4); w1_issue(1, 5); }
    const u32 base = (ch & 3) * STG;
    {
      v4u v = lds128(base + w * 1024 + l * 16);
      s1a = dot2(v.x, ONES, s1a); s2a = dot2(v.x, v.x, s2a);
      s1a = dot2(v.y, ONES, s1a); s2a = dot2(v.y, v.y, s2a);
      s1a = dot2(v.z, ONES, s1a); s2a = dot2(v.z, v.z, s2a);
      s1a = dot2(v.w, ONES, s1a); s2a = dot2(v.w, v.w, s2a);
      if (w < 2) {
        v = lds128(base + (8 + w) * 1024 + l * 16);
        s1b = dot2(v.x, ONES, s1b); s2b = dot2(v.x, v.x, s2b);
        s1b = dot2(v.y, ONES, s1b); s2b = dot2(v.y, v.y, s2b);
        s1b = dot2(v.z, ONES, s1b); s2b = dot2(v.z, v.z, s2b);
        s1b = dot2(v.w, ONES, s1b); s2b = dot2(v.w, v.w, s2b);
      }
      if (ch == 7) {
        auto fin = [&](float s1, float s2, int row) {
          s1 += dppf<0xB1>(s1); s2 += dppf<0xB1>(s2);
          s1 += dppf<0x4E>(s1); s2 += dppf<0x4E>(s2);
          s1 += dppf<0x141>(s1); s2 += dppf<0x141>(s2);
          const float m = s1 * (1.f / 512.f);
          const float var = fmaxf(s2 * (1.f / 512.f) - m * m, 0.f);
          const float r = rsqrtf(var + 1e-5f);
          f2 o = {r, r * m};
          if ((l & 7) == 0) *(LAS f2*)(L_MISC1 + row * 8) = o;
        };
        fin(s1a, s2a, 8 * w + (l >> 3));
        if (w < 2) fin(s1b, s2b, 64 + 8 * w + (l >> 3));
      }
    }
#pragma unroll
    for (int k2 = 0; k2 < 2; ++k2) {
      const u32 xa = base + (xoff ^ (k2 * 64)), wa = base + woff + k2 * 4 * 3072;
      v4u x0 = lds128(xa), x1 = lds128(xa + 2048), x2 = lds128(xa + 4096);
      v4u fq = trfrag(wa), fk = trfrag(wa + 1024), fv = trfrag(wa + 2048);
      if (c == 0) x0 = v4u{ONES, ONES, ONES, ONES};
      aQ1 = mfma32(fq, x1, aQ1);
      aQ2 = mfma32(fq, x2, aQ2);
      aK1 = mfma32(fk, x1, aK1);
      aK2 = mfma32(fk, x2, aK2);
      aV1 = mfma32(x1, fv, aV1);
      aV2 = mfma32(x2, fv, aV2);
      aQ0 = mfma32(fq, x0, aQ0);
      aK0 = mfma32(fk, x0, aK0);
      aV0 = mfma32(x0, fv, aV0);
    }
  }
  BAR();
  STAMP(2);
  {
    const u32 tb = L_MISC1 + (2 * wh) * 128;
    const f2 t0 = *(LAS f2*)(tb + c * 8), t1 = *(LAS f2*)(tb + 128 + c * 8), t2 = *(LAS f2*)(tb + 256 + c * 8);
    f4 csq, csk;
#pragma unroll
    for (int i = 0; i < 4; ++i) {
      csq[i] = dppf<0x150>(aQ0[i]);
      csk[i] = dppf<0x150>(aK0[i]);
    }
    const float csv = __shfl(aV0[0], c);
    auto foldT = [&](f4 acc, f2 t, f4 cs) {
      v2u r;
      r.x = pk(acc[0] * t.x - t.y * cs[0], acc[1] * t.x - t.y * cs[1]);
      r.y = pk(acc[2] * t.x - t.y * cs[2], acc[3] * t.x - t.y * cs[3]);
      return r;
    };
    auto foldV = [&](f4 acc, int j) {
      const f4 u0 = lds128f(tb + j * 128 + g * 32), u1 = lds128f(tb + j * 128 + g * 32 + 16);
      v2u r;
      r.x = pk(acc[0] * u0[0] - u0[1] * csv, acc[1] * u0[2] - u0[3] * csv);
      r.y = pk(acc[2] * u1[0] - u1[1] * csv, acc[3] * u1[2] - u1[3] * csv);
      return r;
    };
    const v2u Q1 = foldT(aQ1, t1, csq), Q2 = foldT(aQ2, t2, csq);
    const v2u K0 = foldT(aK0, t0, csk), K1 = foldT(aK1, t1, csk), K2 = foldT(aK2, t2, csk);
    const v2u V0 = foldV(aV0, 0), V1 = foldV(aV1, 1), V2 = foldV(aV2, 2);
    const bool odd = c & 1;
    const bool cur0 = (4 * g + (c & 1)) <= c, cur1 = (4 * g + 2 + (c & 1)) <= c;
    const f4 z4 = {0, 0, 0, 0};
    auto attend = [&](v2u Q, v2u Kp, v2u Kc, v2u Vp, v2u Vc, bool noprev) {
      const f4 sp = mfma16(Kp, Q, z4), sc = mfma16(Kc, Q, z4);
      const float p0v = odd ? sp[1] : sp[0], p1v = odd ? sp[3] : sp[2];
      const float c0v = odd ? sc[1] : sc[0], c1v = odd ? sc[3] : sc[2];
      float v0 = cur0 ? c0v : (noprev ? -3.0e38f : p0v), v1 = cur1 ? c1v : (noprev ? -3.0e38f : p1v);
      const float mx = gmax(fmaxf(v0, v1));
      const float kk = 0.25f * 1.44269504088896f;
      const float e0 = __builtin_amdgcn_exp2f((v0 - mx) * kk), e1 = __builtin_amdgcn_exp2f((v1 - mx) * kk);
      const u32 pp = pk(e0, e1);
      const float sum = gsum(blo(pp) + bhi(pp));
      const u32 w0 = odd ? (pp << 16) : (pp & 0xffffu), w1 = odd ? (pp & 0xffff0000u) : (pp >> 16);
      const v4u P = {cur0 ? 0u : w0, cur1 ? 0u : w1, cur0 ? w0 : 0u, cur1 ? w1 : 0u};
      const v4u V = {Vp.x, Vp.y, Vc.x, Vc.y};
      const f4 o = mfma32(V, P, z4);
      const float inv = __builtin_amdgcn_rcpf(sum);
      v2u r;
      r.x = pk(o[0] * inv, o[1] * inv);
      r.y = pk(o[2] * inv, o[3] * inv);
      return r;
    };
    const v2u oa = attend(Q1, K0, K1, V0, V1, R == 0 && wh == 0), ob = attend(Q2, K1, K2, V1, V2, false);
    const v4u ov = widen(oa, ob);
    const u32 tt = 4 * R + 2 * wh + (g & 1), ks = 2 * C + (wq_ >> 1);
    st_sc1_128(rws, WS_OF + (tt * 16 + ks) * 1024 + (c + 16 * ((wq_ & 1) * 2 + (g >> 1))) * 16, ov);
  }
  STAMP(3);
  if (p3) {
    w1_issue(2, 6); w1_issue(3, 7); w1_issue(4, 8);
    WAITV(6);
  } else {
    WAITV(0);
  }
  asm volatile("" : "+v"(xres0), "+v"(xres1), "+v"(b1v), "+v"(b2v));
  BAR();
  if (w == 0 && l < 8) st_sc1_32(rws, WS_FLAGA + (R * 8 + l) * 128 + C * 4, tag);
  STAMP(4);
  v2u hp0, hp1;
  {
    v4u wf[8][4];
#pragma unroll
    for (int kk = 0; kk < 8; ++kk)
#pragma unroll
      for (int t = 0; t < 4; ++t) wf[kk][t] = trfrag((8 * wh + kk) * 4096 + troff(t ^ (2 * wh)));
    BAR();
    if (p3) { w1_issue(5, 0); w1_issue(6, 1); }
    if (w == 1) {
      const u32 vo = WS_FLAGA + (R * 8 + C) * 128 + (l & 7) * 4;
      for (u32 sp = 0; sp < SPIN_CAP; ++sp) {
        u32 f;
        asm volatile("buffer_load_dword %0, %1, %2, 0 offen sc1\n\ts_waitcnt vmcnt(0)" : "=v"(f) : "v"(vo), "s"(rws) : "memory");
        if (__all(f == tag)) break;
      }
    }
    BAR();
    STAMP(5);
    v4u of[8];
    {
      const u32 vo = l * 16, so = WS_OF + ((4 * R + wq_) * 16 + 8 * wh) * 1024;
      LD_SC1(of[0], rws, vo, so, 0); LD_SC1(of[1], rws, vo, so, 1024); LD_SC1(of[2], rws, vo, so, 2048); LD_SC1(of[3], rws, vo, so, 3072);
      const u32 so2 = so + 4096;
      LD_SC1(of[4], rws, vo, so2, 0); LD_SC1(of[5], rws, vo, so2, 1024); LD_SC1(of[6], rws, vo, so2, 2048); LD_SC1(of[7], rws, vo, so2, 3072);
    }
    if (p3) { w1_issue(7, 2); w1_issue(8, 3); } else { w2_issue(0, 0); w2_issue(1, 1); }
    f4 acc[4] = {{0, 0, 0, 0}, {0, 0, 0, 0}, {0, 0, 0, 0}, {0, 0, 0, 0}};
#define P2STEP(kk) WAITV1(4 + 7 - kk, of[kk]); _Pragma("unroll") for (int t = 0; t < 4; ++t) acc[t] = mfma32(wf[kk][t], of[kk], acc[t]);
    P2STEP(0) P2STEP(1) P2STEP(2) P2STEP(3) P2STEP(4) P2STEP(5) P2STEP(6) P2STEP(7)
    STAMP(6);
    *(LAS f4*)(L_M + w * 2048 + l * 16) = acc[2];
    *(LAS f4*)(L_M + w * 2048 + 1024 + l * 16) = acc[3];
    BAR();
    const f4 h0 = acc[0] + lds128f(L_M + (w ^ 4) * 2048 + l * 16), h1 = acc[1] + lds128f(L_M + (w ^ 4) * 2048 + 1024 + l * 16);
    hp0.x = pk(h0[0] + blo(xres0.x), h0[1] + bhi(xres0.x));
    hp0.y = pk(h0[2] + blo(xres0.y), h0[3] + bhi(xres0.y));
    hp1.x = pk(h1[0] + blo(xres1.x), h1[1] + bhi(xres1.x));
    hp1.y = pk(h1[2] + blo(xres1.y), h1[3] + bhi(xres1.y));
    float s1 = dot2(hp0.x, ONES, 0.f), s2 = dot2(hp0.x, hp0.x, 0.f);
    s1 = dot2(hp0.y, ONES, s1); s2 = dot2(hp0.y, hp0.y, s2);
    s1 = dot2(hp1.x, ONES, s1); s2 = dot2(hp1.x, hp1.x, s2);
    s1 = dot2(hp1.y, ONES, s1); s2 = dot2(hp1.y, hp1.y, s2);
    s1 = gsum(s1); s2 = gsum(s2);
    if (g < 2) {
      const u32 vo = WS_STATS + (R * 64 + 16 * wq_ + c) * 8 + g * 4;
      const float v = g ? s2 : s1;
      asm volatile("buffer_atomic_add_f32 %0, %1, %2, 0 offen sc1" ::"v"(v), "v"(vo), "s"(rws) : "memory");
    }
    const u32 tb = L_M + (w ^ 4) * 2048;
    asm volatile("s_waitcnt lgkmcnt(0)" ::: "memory");
    *(LAS v2u*)(tb + c * 64 + g * 8) = hp0;
    *(LAS v2u*)(tb + c * 64 + 32 + g * 8) = hp1;
    asm volatile("s_waitcnt lgkmcnt(0)" ::: "memory");
    const u32 ta = tb + (8 * (g >> 1) + q4) * 64 + (g & 1) * 32 + p4 * 8;
    const v2u y0 = trd(ta), y1 = trd(ta + 256);
    const v4u hv = {y0.x, y0.y, y1.x, y1.y};
    const u32 dtile = 4 * C + 2 * wh + (g & 1), ks = 2 * R + (wq_ >> 1);
    st_sc1_128(rws, WS_HF + (dtile * 64 + ks) * 1024 + (c + 16 * ((wq_ & 1) * 2 + (g >> 1))) * 16, hv);
  }
  STAMP(7);
  v4u bw[7][4];
  u32 tro[4];
#pragma unroll
  for (int t = 0; t < 4; ++t) tro[t] = wq_ * 4096 + troff(t ^ wq_);
  f4 acc[4][2];
  if (p3) {
#pragma unroll
    for (int j = 0; j < 7; ++j)
#pragma unroll
      for (int t = 0; t < 4; ++t) bw[j][t] = trfrag(((j < 5) ? (4 + j) : (j - 5)) * 16384 + tro[t]);
    WAITV(0);
    BAR();
    if (w == 0 && l < REPC) st_sc1_32(rws, WS_FLAGC + l * 1024 + b * 4, tag);
    STAMP(8);
#pragma unroll
    for (int j = 9; j < 16; ++j) w1_issue(j, (j < 14) ? (j - 5) : (j - 14));
    if (w == 1) {
      const u32 vo = WS_FLAGC + (b % REPC) * 1024 + l * 16;
      for (u32 sp = 0; sp < SPIN_CAP; ++sp) {
        v4u f;
        asm volatile("buffer_load_dwordx4 %0, %1, %2, 0 offen sc1\n\ts_waitcnt vmcnt(0)" : "=v"(f) : "v"(vo), "s"(rws) : "memory");
        if (__all(f.x == tag && f.y == tag && f.z == tag && f.w == tag)) break;
      }
    }
    BAR();
    STAMP(9);
    if (b == 0 && tid == 0) st_sc1_32(rws, WS_EPOCH, tag);
    v4u st0, st1;
    {
      const u32 vo = tid * 32;
      LD_SC1(st0, rws, vo, WS_STATS + 0u, 0);
      LD_SC1(st1, rws, vo, WS_STATS + 0u, 16);
    }
    constexpr int PF = 5;
    v4u af[16][2];
    const u32 avo = l * 16;
    const u32 abase = WS_HF + ((4 * C + 2 * wh) * 64 + 16 * wq_) * 1024;
#define A3LOAD(n) { const u32 so = abase + (((n) + 7 + rot3) & 15) * 1024; LD_SC1(af[n][0], rws, avo, so, 0); const u32 so2 = so + 65536; LD_SC1(af[n][1], rws, avo, so2, 0); }
    A3LOAD(0) A3LOAD(1) A3LOAD(2) A3LOAD(3) A3LOAD(4)
    WAITV2(2 * PF, st0, st1);
    {
      auto fin = [&](u32 a1, u32 a2, float& r, float& cc) {
        const float m = __uint_as_float(a1) * (1.f / 512.f);
        const float var = fmaxf(__uint_as_float(a2) * (1.f / 512.f) - m * m, 0.f);
        const float r1 = rsqrtf(var + 1e-5f);
        const float r2 = rsqrtf(var * r1 * r1 + 1e-5f);
        r = r1 * r2;
        cc = -m * r;
      };
      f4 rr, cc;
      float r, c_;
      fin(st0.x, st0.y, r, c_); rr[0] = r; cc[0] = c_;
      fin(st0.z, st0.w, r, c_); rr[1] = r; cc[1] = c_;
      fin(st1.x, st1.y, r, c_); rr[2] = r; cc[2] = c_;
      fin(st1.z, st1.w, r, c_); rr[3] = r; cc[3] = c_;
      *(LAS f4*)(L_M + tid * 16) = rr;
      *(LAS f4*)(L_M + 8192 + tid * 16) = cc;
    }
    BAR();
#pragma unroll
    for (int t = 0; t < 4; ++t) acc[t][0] = acc[t][1] = f4{0, 0, 0, 0};
    auto norm = [&](v4u v, f4 r0v, f4 r1v, f4 c0v, f4 c1v) {
      v4u o;
      o.x = pk(blo(v.x) * r0v[0] + c0v[0], bhi(v.x) * r0v[1] + c0v[1]);
      o.y = pk(blo(v.y) * r0v[2] + c0v[2], bhi(v.y) * r0v[3] + c0v[3]);
      o.z = pk(blo(v.z) * r1v[0] + c1v[0], bhi(v.z) * r1v[1] + c1v[1]);
      o.w = pk(blo(v.w) * r1v[2] + c1v[2], bhi(v.w) * r1v[3] + c1v[3]);
      return o;
    };
    const u32 tbase = L_M + (16 * wq_ * 32 + 8 * g) * 4;
#define P3STEP(n, NW, BF)                                                                        \
  {                                                                                              \
    const u32 ta = tbase + (((n) + 7 + rot3) & 15) * 128;                                        \
    const f4 r0v = lds128f(ta), r1v = lds128f(ta + 16), c0v = lds128f(ta + 8192), c1v = lds128f(ta + 8192 + 16); \
    WAITV2(NW, af[n][0], af[n][1]);                                                              \
    const v4u h0 = norm(af[n][0], r0v, r1v, c0v, c1v), h1 = norm(af[n][1], r0v, r1v, c0v, c1v);  \
    _Pragma("unroll") for (int t = 0; t < 4; ++t) {                                              \
      const v4u bf = BF;                                                                         \
      acc[t][0] = mfma32(bf, h0, acc[t][0]);                                                     \
      acc[t][1] = mfma32(bf, h1, acc[t][1]);                                                     \
    }                                                                                            \
  }
#define SL3(n) (((n) < 7) ? ((n) + 2) : ((n)-7))
    P3STEP(0, 2 * (PF - 1), trfrag(SL3(0) * 16384 + tro[t])) A3LOAD(5)
    P3STEP(1, 2 * (PF - 1), trfrag(SL3(1) * 16384 + tro[t])) A3LOAD(6)
    P3STEP(2, 2 * (PF - 1), trfrag(SL3(2) * 16384 + tro[t])) A3LOAD(7)
    P3STEP(3, 2 * (PF - 1), trfrag(SL3(3) * 16384 + tro[t])) A3LOAD(8)
    P3STEP(4, 2 * (PF - 1), trfrag(SL3(4) * 16384 + tro[t])) A3LOAD(9)
    P3STEP(5, 2 * (PF - 1), trfrag(SL3(5) * 16384 + tro[t])) A3LOAD(10)
    P3STEP(6, 2 * (PF - 1), trfrag(SL3(6) * 16384 + tro[t])) A3LOAD(11)
    P3STEP(7, 2 * (PF - 1), trfrag(SL3(7) * 16384 + tro[t])) A3LOAD(12)
    P3STEP(8, 2 * (PF - 1), trfrag(SL3(8) * 16384 + tro[t])) A3LOAD(13) A3LOAD(14) A3LOAD(15)
    BAR();
#pragma unroll
    for (int j = 0; j < 6; ++j) w2_issue(j, j);
    P3STEP(9, 12 + 12, bw[0][t])
    P3STEP(10, 12 + 10, bw[1][t])
    P3STEP(11, 12 + 8, bw[2][t])
    P3STEP(12, 12 + 6, bw[3][t])
    P3STEP(13, 12 + 4, bw[4][t])
    P3STEP(14, 12 + 2, bw[5][t])
    P3STEP(15, 12 + 0, bw[6][t])
    STAMP(10);
    const u32 rb = 6 * 16384;
#pragma unroll
    for (int t = 1; t < 4; ++t)
#pragma unroll
      for (int m = 0; m < 2; ++m) *(LAS f4*)(rb + (w * 6 + (t - 1) * 2 + m) * 1024 + l * 16) = acc[t][m];
    BAR();
    f4 u0 = acc[0][0], u1 = acc[0][1];
#pragma unroll
    for (int t = 1; t < 4; ++t) {
      u0 += lds128f(rb + ((w ^ t) * 6 + (t - 1) * 2) * 1024 + l * 16);
      u1 += lds128f(rb + ((w ^ t) * 6 + (t - 1) * 2 + 1) * 1024 + l * 16);
    }
    const f4 bb = {blo(b1v.x), bhi(b1v.x), blo(b1v.y), bhi(b1v.y)};
    auto gelu = [&](float v) {
      const float z = v * (1.f + 0.044715f * v * v) * (-2.f * 0.7978845608028654f * 1.44269504088896f);
      return v * __builtin_amdgcn_rcpf(1.f + __builtin_amdgcn_exp2f(z));
    };
    u0 += bb;
    u1 += bb;
    v2u ua, ub;
    ua.x = pk(gelu(u0[0]), gelu(u0[1])); ua.y = pk(gelu(u0[2]), gelu(u0[3]));
    ub.x = pk(gelu(u1[0]), gelu(u1[1])); ub.y = pk(gelu(u1[2]), gelu(u1[3]));
    const v4u uv = widen(ua, ub);
    const u32 dtile = 4 * C + 2 * wh + (g & 1), ks = 2 * FT + (wq_ >> 1);
    st_sc1_128(rws, WS_UF + (dtile * 56 + ks) * 1024 + (c + 16 * ((wq_ & 1) * 2 + (g >> 1))) * 16, uv);
    STAMP(11);
    WAITV(0);
    BAR();
    if (w == 0 && l < 32) st_sc1_32(rws, WS_FLAGD + (l * 8 + C) * 128 + FT * 4, tag);
  } else {
    WAITV(0);
    BAR();
    if (w == 0 && l < REPC) st_sc1_32(rws, WS_FLAGC + l * 1024 + b * 4, tag);
#pragma unroll
    for (int j = 2; j < 6; ++j) w2_issue(j, j);
    WAITV(0);
    BAR();
  }
  STAMP(12);
#pragma unroll
  for (int j = 0; j < 5; ++j)
#pragma unroll
    for (int t = 0; t < 4; ++t) bw[j][t] = trfrag(j * 16384 + tro[t]);
  BAR();
#pragma unroll
  for (int j = 6; j < 14; ++j) w2_issue(j, (j < 9) ? j : (j - 9));
  if (w == 1) {
    const u32 vo = WS_FLAGD + (R * 8 + C) * 128 + (l < 28 ? l : 0) * 4;
    for (u32 sp = 0; sp < SPIN_CAP; ++sp) {
      u32 f;
      asm volatile("buffer_load_dword %0, %1, %2, 0 offen sc1\n\ts_waitcnt vmcnt(0)" : "=v"(f) : "v"(vo), "s"(rws) : "memory");
      if (__all(f == tag)) break;
    }
  }
  BAR();
  STAMP(13);
  {
    v4u af[14][2];
    const u32 avo = l * 16;
    const u32 abase = WS_UF + ((4 * C + 2 * wh) * 56) * 1024;
#define A4LOAD(n) { const u32 so = abase + ks4(n) * 1024; LD_SC1(af[n][0], rws, avo, so, 0); const u32 so2 = so + 56 * 1024; LD_SC1(af[n][1], rws, avo, so2, 0); }
    A4LOAD(0) A4LOAD(1) A4LOAD(2) A4LOAD(3) A4LOAD(4) A4LOAD(5) A4LOAD(6) A4LOAD(7)
#pragma unroll
    for (int t = 0; t < 4; ++t) acc[t][0] = acc[t][1] = f4{0, 0, 0, 0};
#define P4STEP(n, NW, BF)                              \
  {                                                    \
    WAITV2(NW, af[n][0], af[n][1]);                    \
    _Pragma("unroll") for (int t = 0; t < 4; ++t) {    \
      const v4u bf = BF;                               \
      acc[t][0] = mfma32(af[n][0], bf, acc[t][0]);     \
      acc[t][1] = mfma32(af[n][1], bf, acc[t][1]);     \
    }                                                  \
  }
#define SL4(n) (((n) < 9) ? (n) : ((n)-9))
    P4STEP(0, 14, bw[0][t]) A4LOAD(8)
    BAR();
    P4STEP(1, 14, bw[1][t]) A4LOAD(9)
    P4STEP(2, 14, bw[2][t]) A4LOAD(10)
    P4STEP(3, 14, bw[3][t]) A4LOAD(11)
    P4STEP(4, 14, bw[4][t]) A4LOAD(12)
    P4STEP(5, 14, trfrag(SL4(5) * 16384 + tro[t])) A4LOAD(13)
    P4STEP(6, 14, trfrag(SL4(6) * 16384 + tro[t]))
    P4STEP(7, 12, trfrag(SL4(7) * 16384 + tro[t]))
    P4STEP(8, 10, trfrag(SL4(8) * 16384 + tro[t]))
    P4STEP(9, 8, trfrag(SL4(9) * 16384 + tro[t]))
    P4STEP(10, 6, trfrag(SL4(10) * 16384 + tro[t]))
    P4STEP(11, 4, trfrag(SL4(11) * 16384 + tro[t]))
    P4STEP(12, 2, trfrag(SL4(12) * 16384 + tro[t]))
    P4STEP(13, 0, trfrag(SL4(13) * 16384 + tro[t]))
    STAMP(14);
    BAR();
#pragma unroll
    for (int t = 1; t < 4; ++t)
#pragma unroll
      for (int m = 0; m < 2; ++m) *(LAS f4*)((w * 6 + (t - 1) * 2 + m) * 1024 + l * 16) = acc[t][m];
    BAR();
    f4 y0 = acc[0][0], y1 = acc[0][1];
#pragma unroll
    for (int t = 1; t < 4; ++t) {
      y0 += lds128f(((w ^ t) * 6 + (t - 1) * 2) * 1024 + l * 16);
      y1 += lds128f(((w ^ t) * 6 + (t - 1) * 2 + 1) * 1024 + l * 16);
    }
    const float bs = blo(b2v);
    v2u oa, ob;
    oa.x = pk(y0[0] + bs + blo(hp0.x), y0[1] + bs + bhi(hp0.x));
    oa.y = pk(y0[2] + bs + blo(hp0.y), y0[3] + bs + bhi(hp0.y));
    ob.x = pk(y1[0] + bs + blo(hp1.x), y1[1] + bs + bhi(hp1.x));
    ob.y = pk(y1[2] + bs + blo(hp1.y), y1[3] + bs + bhi(hp1.y));
    const v4u ov = widen(oa, ob);
    *(v4u*)((char*)a.out + (size_t)(R * 64 + 16 * wq_ + c) * 1024 + (64 * C + 32 * wh + 16 * (g & 1) + 8 * (g >> 1)) * 2) = ov;
  }
  STAMP(15);
  {
    const u32 tgt = 6000u + (p3 ? (stamp[ENC] - stamp[0]) : 0u);
    for (;;) { asm volatile("s_memrealtime %0\n\ts_waitcnt lgkmcnt(0)" : "=s"(stp) :: "memory"); if ((u32)stp - stamp[0] >= tgt) break; __builtin_amdgcn_s_sleep(2); }
  }
  if (tid == 0) {
    if (!p3) { stamp[8] = stamp[9] = stamp[10] = stamp[11] = 0; }
#pragma unroll
    for (int i = 0; i < 16; ++i) ((u32*)((char*)a.ws + WS_STAMP))[b * 16 + i] = stamp[i];
  }
}

extern "C" void kernel_launch(void* const* d_in, const int* in_sizes, int n_in, void* d_out, int out_size, void* d_ws, size_t ws_size, hipStream_t stream) {
  Args a;
  a.x = d_in[0];
  a.wqkv = d_in[1];
  a.wo = d_in[2];
  a.w1 = d_in[4];
  a.b1 = d_in[5];
  a.w2 = d_in[6];
  a.b2 = d_in[7];
  a.out = d_out;
  a.ws = d_ws;
  (void)hipFuncSetAttribute((const void*)fused_kernel, hipFuncAttributeMaxDynamicSharedMemorySize, LDS_BYTES);
  fused_kernel<<<256, 512, LDS_BYTES, stream>>>(a);
}
```

```cpp
#include <hip/hip_runtime.h>
#include <hip/hip_bf16.h>

typedef unsigned u32;
typedef unsigned long long u64;
typedef u32 v4u __attribute__((ext_vector_type(4)));
typedef u32 v2u __attribute__((ext_vector_type(2)));
typedef float f4 __attribute__((ext_vector_type(4)));
typedef float f2 __attribute__((ext_vector_type(2)));
typedef __bf16 v8bf __attribute__((ext_vector_type(8)));
typedef __bf16 v2bf __attribute__((ext_vector_type(2)));
typedef short v4s __attribute__((ext_vector_type(4)));
#define LAS __attribute__((address_space(3)))
#define DI __device__ __forceinline__

#define XMASK 1
#define XOFF 1
#define PRIO 1
#define AX (((bi >> 2) & 1) * 2)
#define ABL 0
#define STAMPS 0

constexpr int S_ = 2048, D_ = 512, F_ = 1792;
constexpr u32 WS_EPOCH = 0, WS_FLAGA = 4096, WS_FLAGD = WS_FLAGA + 32768, WS_FLAGC = WS_FLAGD + 32768;
constexpr u32 WS_SX1 = WS_FLAGC + 32768;
constexpr u32 WS_STAMP = 7u << 20, WS_SX = 512 * 1024;
constexpr u32 WS_OF = 1u << 20, WS_HF = 3u << 20, WS_UF = 5u << 20, WS_BYTES = 8u << 20;
constexpr u32 STG = 34816, L_MISC1 = 152 * 1024, L_M = 144 * 1024, LDS_BYTES = 160 * 1024;
constexpr u32 SPIN_CAP = 1u << 16;
constexpr u32 CODE_BYTES = 30000;

DI v4u mkrs(const void* p, u32 bytes) {
  v4u r;
  r.x = (u32)(size_t)p;
  r.y = (u32)((size_t)p >> 32) & 0xffffu;
  r.z = bytes;
  r.w = 0x00020000u;
  return r;
}
#define dma1(rs, voff, soff, m0b, imm) \
  asm volatile("s_add_u32 m0, %3, %4\n\ts_nop 0\n\tbuffer_load_dwordx4 %0, %1, %2 offen lds" ::"v"(voff), "s"(rs), "s"(soff), "s"(m0b), "i"(imm) : "memory", "scc")
#define dma2(rs, voffa, voffb, soff, m0b, imm)                                                                                                        \
  asm volatile("s_add_u32 m0, %4, %5\n\ts_nop 0\n\tbuffer_load_dwordx4 %0, %2, %3 offen lds\n\tbuffer_load_dwordx4 %1, %2, %3 offen offset:1024 lds" ::"v"(voffa), \
               "v"(voffb), "s"(rs), "s"(soff), "s"(m0b), "i"(imm)                                                                                     \
               : "memory", "scc")
#define dma3(rs, voff, soff, m0b, imm)                                                                                                               \
  asm volatile(                                                                                                                                      \
      "s_add_u32 m0, %3, %4\n\ts_nop 0\n\tbuffer_load_dwordx4 %0, %1, %2 offen lds\n\tbuffer_load_dwordx4 %0, %1, %2 offen offset:1024 lds\n\tbuffer_load_dwordx4 %0, %1, %2 offen offset:2048 lds" ::"v"(voff), \
      "s"(rs), "s"(soff), "s"(m0b), "i"(imm)                                                                                                         \
      : "memory", "scc")
#define dma1o(rs, voff, soff, m0b, imm, off) \
  asm volatile("s_add_u32 m0, %3, %4\n\ts_nop 0\n\tbuffer_load_dwordx4 %0, %1, %2 offen offset:%5 lds" ::"v"(voff), "s"(rs), "s"(soff), "s"(m0b), "i"(imm), "i"(off) : "memory", "scc")
#define dma1m(rs, voff, soff, m0b, imm, mask)                                                                                                   \
  asm volatile("s_add_u32 m0, %3, %4\n\ts_mov_b64 exec, %5\n\tbuffer_load_dwordx4 %0, %1, %2 offen lds\n\ts_mov_b64 exec, -1" ::"v"(voff), "s"(rs), \
               "s"(soff), "s"(m0b), "i"(imm), "s"(mask)                                                                                         \
               : "memory", "scc")
#define LD_SC1(dst, rs, voff, soff, imm) asm volatile("buffer_load_dwordx4 %0, %1, %2, %3 offen offset:%4 sc1" : "=v"(dst) : "v"(voff), "s"(rs), "s"(soff), "i"(imm) : "memory")
#define WAITV(n) asm volatile("s_waitcnt vmcnt(%0)" ::"i"(n) : "memory")
#define WAITV1(n, a) asm volatile("s_waitcnt vmcnt(%1)" : "+v"(a) : "i"(n) : "memory")
#define WAITV2(n, a, b) asm volatile("s_waitcnt vmcnt(%2)" : "+v"(a), "+v"(b) : "i"(n) : "memory")
#define SB() __builtin_amdgcn_sched_barrier(0)
#define LAUNDER(x) asm volatile("" : "+v"(x))
#define BAR() asm volatile("s_waitcnt lgkmcnt(0)\n\ts_barrier" ::: "memory")
DI void st_sc1_128(v4u rs, u32 voff, v4u v) { asm volatile("buffer_store_dwordx4 %0, %1, %2, 0 offen sc1\n\ts_nop 1" ::"v"(v), "v"(voff), "s"(rs) : "memory"); }
#define ST32M(rs, voff, v, mask) asm volatile("s_mov_b64 exec, %3\n\tbuffer_store_dword %0, %1, %2, 0 offen sc1\n\ts_mov_b64 exec, -1" ::"v"(v), "v"(voff), "s"(rs), "i"(mask) : "memory")
#define ST128M(rs, voff, v, mask) asm volatile("s_mov_b64 exec, %3\n\tbuffer_store_dwordx4 %0, %1, %2, 0 offen sc1\n\ts_mov_b64 exec, -1\n\ts_nop 0" ::"v"(v), "v"(voff), "s"(rs), "i"(mask) : "memory")
DI void st_sc1_32(v4u rs, u32 voff, u32 v) { asm volatile("buffer_store_dword %0, %1, %2, 0 offen sc1" ::"v"(v), "v"(voff), "s"(rs) : "memory"); }

DI f4 mfma32(v4u a, v4u b, f4 c) { return __builtin_amdgcn_mfma_f32_16x16x32_bf16(__builtin_bit_cast(v8bf, a), __builtin_bit_cast(v8bf, b), c, 0, 0, 0); }
DI f4 mfma16(v2u a, v2u b, f4 c) { return __builtin_amdgcn_mfma_f32_16x16x16bf16_1k(__builtin_bit_cast(v4s, a), __builtin_bit_cast(v4s, b), c, 0, 0, 0); }
DI u32 pk(float a, float b) {
  f2 v = {a, b};
  return __builtin_bit_cast(u32, __builtin_convertvector(v, v2bf));
}
DI u32 pk2(f2 v) { return __builtin_bit_cast(u32, __builtin_convertvector(v, v2bf)); }
DI f2 lo2(f4 v) { return __builtin_shufflevector(v, v, 0, 1); }
DI f2 hi2(f4 v) { return __builtin_shufflevector(v, v, 2, 3); }
DI f2 fma2(f2 a, f2 b, f2 c) { return __builtin_elementwise_fma(a, b, c); }
DI f2 bc2(float x) { return f2{x, x}; }
DI float blo(u32 v) { return __uint_as_float(v << 16); }
DI float bhi(u32 v) { return __uint_as_float(v & 0xffff0000u); }
DI float dot2(u32 a, u32 b, float c) { return __builtin_amdgcn_fdot2_f32_bf16(__builtin_bit_cast(v2bf, a), __builtin_bit_cast(v2bf, b), c, false); }
DI v4u lds128(u32 a) { return *(LAS v4u*)a; }
DI f4 lds128f(u32 a) { return *(LAS f4*)a; }
DI v2u trd(u32 a) { return __builtin_bit_cast(v2u, __builtin_amdgcn_ds_read_tr16_b64_v4i16((LAS v4s*)a)); }
DI v4u trfrag(u32 a) {
  v2u x = trd(a), y = trd(a + 512);
  v4u r = {x.x, x.y, y.x, y.y};
  return r;
}
template <int CTRL> DI float dppf(float x) { return __uint_as_float(__builtin_amdgcn_mov_dpp(__float_as_uint(x), CTRL, 0xf, 0xf, true)); }
DI float gsum(float x) {
  auto a = __builtin_amdgcn_permlane16_swap(__float_as_uint(x), __float_as_uint(x), false, false);
  x = __uint_as_float(a[0]) + __uint_as_float(a[1]);
  auto b = __builtin_amdgcn_permlane32_swap(__float_as_uint(x), __float_as_uint(x), false, false);
  return __uint_as_float(b[0]) + __uint_as_float(b[1]);
}
DI float fmx(float a, float b) { return __builtin_elementwise_maximum(a, b); }
DI float sw16max(float x) {
  auto a = __builtin_amdgcn_permlane16_swap(__float_as_uint(x), __float_as_uint(x), false, false);
  return fmx(__uint_as_float(a[0]), __uint_as_float(a[1]));
}
DI float sw32max(float x) {
  auto a = __builtin_amdgcn_permlane32_swap(__float_as_uint(x), __float_as_uint(x), false, false);
  return fmx(__uint_as_float(a[0]), __uint_as_float(a[1]));
}
DI float sw16add(float x) {
  auto a = __builtin_amdgcn_permlane16_swap(__float_as_uint(x), __float_as_uint(x), false, false);
  return __uint_as_float(a[0]) + __uint_as_float(a[1]);
}
DI float sw32add(float x) {
  auto a = __builtin_amdgcn_permlane32_swap(__float_as_uint(x), __float_as_uint(x), false, false);
  return __uint_as_float(a[0]) + __uint_as_float(a[1]);
}
DI float wsum(float x) {
  x += dppf<0xB1>(x);
  x += dppf<0x4E>(x);
  x += dppf<0x141>(x);
  x += dppf<0x140>(x);
  return gsum(x);
}
DI void wsum2(float& x, float& y) {
  x += dppf<0xB1>(x); y += dppf<0xB1>(y);
  x += dppf<0x4E>(x); y += dppf<0x4E>(y);
  x += dppf<0x141>(x); y += dppf<0x141>(y);
  x += dppf<0x140>(x); y += dppf<0x140>(y);
  x = sw16add(x); y = sw16add(y);
  x = sw32add(x); y = sw32add(y);
}
DI v4u widen(v2u a, v2u b) {
  auto x = __builtin_amdgcn_permlane16_swap(a.x, b.x, false, false);
  auto y = __builtin_amdgcn_permlane16_swap(a.y, b.y, false, false);
  v4u r = {x[0], y[0], x[1], y[1]};
  return r;
}

#define STAMP(i)

#define TS(k)
#define TSFIN(n)
struct Args {
  const void *x, *wqkv, *wo, *w1, *b1, *w2, *b2;
  void *out, *ws;
};

__global__ __launch_bounds__(512, 2) void fused_kernel(Args a) {
  STAMP(0);
  const int tid = threadIdx.x, l = tid & 63, w = __builtin_amdgcn_readfirstlane(tid >> 6);
  const int b = blockIdx.x, xcd = b & 7, bi = b >> 3;
  {
    u64 pc;
    asm volatile("s_getpc_b64 %0" : "=s"(pc));
    const v4u rc = mkrs((const void*)(pc & ~127ull), CODE_BYTES);
    asm volatile("s_mov_b32 m0, %2\n\ts_mov_b64 exec, 1\n\tbuffer_load_dword off, %0, %1 lds\n\ts_mov_b64 exec, -1" ::"s"(rc), "s"((bi * 8 + w) * 128), "s"(L_MISC1 + 4096));
  }
  const int c = l & 15, g = l >> 4, q4 = (l & 15) >> 2, p4 = l & 3;
  const int wq_ = w & 3, wh = w >> 2;
  const int R = 8 * (xcd >> 1) + (bi >> 2), C = 4 * (xcd & 1) + (bi & 3);
  const int FT = 7 * (xcd >> 1) + (bi >> 2);
  const bool p3 = __builtin_expect((bi >> 2) < 7, 1);
  u32 avo = l * 16;
  u32 vxa = (l >> 3) * 1024 + (((l & 7) ^ (l >> 3)) << 4);
  u32 vxh = vxa ^ ((wq_ & 1) << 6);
  u32 vwb = (8 * w + (l >> 3)) * 3072 + (((l & 7) ^ ((((l >> 4) & 1) << 1) | ((w & 1) << 2))) << 4);
  const int r0 = R * 64 - 16;
  u32 xrow_a = (u32)max(r0 + 16 * wq_, 0) * 1024u, xrow_b = xrow_a + 8192u, xrow_h = (u32)(r0 + 64 + 4 * wq_) * 1024u;
  const int xr1 = ((bi >> 2) + (XMASK == 7 ? 2 : 1) * (bi & 3)) & XMASK, xo1 = (XOFF * ((xcd >> 1) * 2 + (xcd & 1) * 4)) & 7;
  u32 mx2k = wq_ * 2048, mxh = wq_ * 512, mw3k = w * 3072, mw1k = w * 1024;
  const u64 mask0 = (wq_ == 0) ? ~0xFFull : ~0ull;
  const u64 maskh = 0xFFFFFFFFull;
#define KC(ch) ((((ch) ^ xr1) + xo1) & 7)
#define stage_xa(ch) dma1m(rx, vxa, xrow_a + KC(ch) * 128, mx2k, ((ch)&3) * STG, mask0)
#define stage_xb(ch) dma1(rx, vxa, xrow_b + KC(ch) * 128, mx2k, ((ch)&3) * STG + 1024)
#define stage_xh(ch) dma1m(rx, vxh, xrow_h + KC(ch) * 128, mxh, ((ch)&3) * STG + 8192, maskh)
#define stage_x(ch) { stage_xa(ch); stage_xb(ch); stage_xh(ch); }
#define stage_w(ch) dma3(rqkv, vwb, KC(ch) * (64 * 3072) + C * 128, mw3k, ((ch)&3) * STG + 10240)
#define stage_w1(ch, i) dma1o(rqkv, vwb, KC(ch) * (64 * 3072) + C * 128, mw3k, ((ch)&3) * STG + 10240, (i)*1024)
  const u32 ONES = 0x3F803F80u;
  u32 xrso = (R * 64 + 8 * C + w) * 1024, kc0x = KC(0) * 128, kc0w = KC(0) * (64 * 3072) + C * 128;
  asm volatile("" : "+v"(avo), "+v"(vxa), "+v"(vxh), "+v"(vwb));
  asm volatile("" : "+s"(xrow_a), "+s"(xrow_b), "+s"(xrow_h), "+s"(mx2k), "+s"(mxh), "+s"(mw3k), "+s"(xrso), "+s"(kc0x), "+s"(kc0w));
  SB();
  const v4u rx = mkrs(a.x, S_ * D_ * 2), rqkv = mkrs(a.wqkv, D_ * 1536 * 2), rwo = mkrs(a.wo, D_ * D_ * 2), rw1 = mkrs(a.w1, (ABL & 16) ? 0 : S_ * F_ * 2),
            rw2 = mkrs(a.w2, (ABL & 16) ? 0 : F_ * S_ * 2), rws = mkrs(a.ws, WS_BYTES);
  u32 tag;
  asm volatile("s_load_dword %0, %1, 0x0" : "=s"(tag) : "s"(a.ws));

  if (wh == 1) {
    dma1m(rx, vxa, xrow_a + kc0x, mx2k, 0, mask0);
    dma1(rx, vxa, xrow_b + kc0x, mx2k, 1024);
    dma1m(rx, vxh, xrow_h + kc0x, mxh, 8192, maskh);
  }
  v4u xr;
  asm volatile("buffer_load_dwordx4 %0, %1, %2, %3 offen" : "=v"(xr) : "v"(avo), "s"(rx), "s"(xrso) : "memory");
  dma3(rqkv, vwb, kc0w, mw3k, 10240);
  STAMP(21);
  if (tid < 32) *(LAS v4u*)((tid >> 3) * STG + (tid & 7) * 16) = v4u{ONES, ONES, ONES, ONES};

  u32 vw1a, vw1b, vw2a, vw2b, vwoa, vwob;
#define PRE_W()                                                                                        \
  {                                                                                                    \
    u32 lr = l >> 3;                                                                                   \
    LAUNDER(lr);                                                                                       \
    const u32 vsw = ((l & 7) ^ (lr & 2)) << 4;                                                         \
    vw1a = lr * 3584 + vsw; vw1b = (vw1a ^ 64u) + 8 * 3584 - 1024;                                     \
    vw2a = lr * 4096 + vsw; vw2b = (vw2a ^ 64u) + 8 * 4096 - 1024;                                     \
    vwoa = lr * 1024 + vsw; vwob = (vwoa ^ 64u) + 8 * 1024 - 1024;                                     \
    LAUNDER(vw1a); LAUNDER(vw1b); LAUNDER(vw2a); LAUNDER(vw2b); LAUNDER(vwoa); LAUNDER(vwob);          \
  }
  const u32 fl = ((q4 >> 1) << 1) | ((g & 1) << 2);
  const u32 trl = (fl + (p4 >> 1)) * 16 + 8 * (p4 & 1);
  u32 k3lo, k3hi, k4lo, k4hi, w1q, w2q, w1h, w2h;
#define PRE_K()                                                                                                            \
  {                                                                                                                        \
    u32 bb = b;                                                                                                            \
    asm volatile("" : "+s"(bb));                                                                                           \
    const u32 bi = bb >> 3, ax = AX, r3 = 4 * (bi & 3) + 2 * (bb & 1), r4 = r3 >= 14 ? r3 - 14 : r3;                       \
    const u64 q3 = 0xFEDCBA9876543210ull ^ (ax * 0x1111111111111111ull);                                                   \
    const u64 t3 = (q3 >> (4 * r3)) | ((q3 << 1) << (63 - 4 * r3));                                                        \
    const u64 q4_ = 0x00DCBA9876543210ull ^ ((ax & 3) * 0x0000111111111111ull) ^ ((u64)(ax & 1) * 0x0011000000000000ull);  \
    const u64 t4 = (q4_ >> (4 * r4)) | (q4_ << (56 - 4 * r4));                                                             \
    k3lo = (u32)t3; k3hi = (u32)(t3 >> 32); k4lo = (u32)t4; k4hi = (u32)(t4 >> 32);                                        \
    w1q = (16 * wq_ * 32) * 3584 + FT * 128; w2q = (14 * wq_ * 32) * 4096 + R * 128;                                       \
    w1h = w1q + 16 * wh * 3584; w2h = w2q + 16 * wh * 4096;                                                                \
    asm volatile("" : "+s"(k3lo), "+s"(k3hi), "+s"(k4lo), "+s"(k4hi), "+s"(w1q), "+s"(w2q), "+s"(w1h), "+s"(w2h));         \
  }
#define KS3(j) ((((j) < 8 ? k3lo : k3hi) >> (4 * ((j)&7))) & 15u)
#define KS4(j) ((((j) < 8 ? k4lo : k4hi) >> (4 * ((j)&7))) & 15u)
  const u32 mslot = wq_ * 4096 + wh * 2048, mw4k = w * 4096, mq4k = wq_ * 4096;
#define w1_issue(j, slot) dma2(rw1, vw1a, vw1b, w1h + KS3(j) * (32 * 3584), mslot, (slot)*16384)
#define w2_issue(j, slot) dma2(rw2, vw2a, vw2b, w2h + KS4(j) * (32 * 4096), mslot, (slot)*16384)
#define w1_issue4(j, slot) { const u32 so_ = w1q + KS3(j) * (32 * 3584); dma2(rw1, vw1a, vw1b, so_, mq4k, (slot)*16384); dma2(rw1, vw1a, vw1b, so_ + 16 * 3584, mq4k, (slot)*16384 + 2048); }
#define w2_issue4(j, slot) { const u32 so_ = w2q + KS4(j) * (32 * 4096); dma2(rw2, vw2a, vw2b, so_, mq4k, (slot)*16384); dma2(rw2, vw2a, vw2b, so_ + 16 * 4096, mq4k, (slot)*16384 + 2048); }
#define wo_issue(half, part) dma2(rwo, vwoa, vwob, ((8 * (half) + w) * 32 + 16 * (part)) * 1024 + C * 128, mw4k, (half)*32768 + (part)*2048)
#define POLL(vo_)                                                                                                                       \
  _Pragma("nounroll") for (u32 sp = 0; sp < SPIN_CAP; ++sp) {                                                                           \
    u32 f;                                                                                                                              \
    asm volatile("buffer_load_dword %0, %1, %2, 0 offen sc1\n\ts_waitcnt vmcnt(0)" : "=v"(f) : "v"(vo_), "s"(rws) : "memory");         \
    if (__all(f == tag)) break;                                                                                                         \
  }
  u32 st_o, st_h, st_u;
#define PRE_ST()                                                                                        \
  {                                                                                                     \
    u32 ll = l;                                                                                         \
    LAUNDER(ll);                                                                                        \
    const u32 fso = ((ll & 15) + 16 * ((wq_ & 1) * 2 + (ll >> 5))) * 16, gp = (ll >> 4) & 1;            \
    st_o = WS_OF + ((4 * R + 2 * wh + gp) * 16 + 2 * C + (wq_ >> 1)) * 1024 + fso;                      \
    st_h = WS_HF + ((4 * C + 2 * wh + gp) * 64 + 2 * R + (wq_ >> 1)) * 1024 + fso;                      \
    st_u = WS_UF + ((4 * C + 2 * wh + gp) * 56 + 2 * FT + (wq_ >> 1)) * 1024 + fso;                     \
    LAUNDER(st_o); LAUNDER(st_h); LAUNDER(st_u);                                                        \
  }

  f4 aQ0 = {0, 0, 0, 0}, aQ1 = aQ0, aQ2 = aQ0, aK0 = aQ0, aK1 = aQ0, aK2 = aQ0, aV0 = aQ0, aV1 = aQ0, aV2 = aQ0;
  constexpr u32 T_CS = L_MISC1 + 1024;
  constexpr u32 L_XRES = 4 * STG;
  const bool odd = c & 1;
  const bool cur0 = (4 * g + (c & 1)) <= c, cur1 = (4 * g + 2 + (c & 1)) <= c;
  float ml0, ml1, ml2, rq1, rq2, fa0, fa1, fb0, fb1;
  f2 rka, rkb;
  f4 mv0, mv1, mv2;
  v2u b1v;
  u32 b2v;
  u32 sel0, sel1, sel2, sel3, csa, kva, csv_a;
  float na0, na1;
#define PRE_SEL()                                                                                                          \
  {                                                                                                                        \
    const u32 z = 0x0c0c0c0cu;                                                                                             \
    u32 s0 = odd ? 0x01000c0cu : 0x0c0c0100u, s1 = odd ? 0x03020c0cu : 0x0c0c0302u;                                        \
    LAUNDER(s0); LAUNDER(s1);                                                                                              \
    sel0 = cur0 ? z : s0; sel1 = cur1 ? z : s1; sel2 = cur0 ? s0 : z; sel3 = cur1 ? s1 : z;                                \
    float nv = (R == 0 && wh == 0) ? -3.0e38f : 0.f;                                                                       \
    LAUNDER(nv);                                                                                                           \
    na0 = cur0 ? 0.f : nv; na1 = cur1 ? 0.f : nv;                                                                          \
    LAUNDER(sel0); LAUNDER(sel1); LAUNDER(sel2); LAUNDER(sel3); LAUNDER(na0); LAUNDER(na1);                                \
  }
#define PRE_TAB()                                                                                                          \
  {                                                                                                                        \
    u32 ll = l;                                                                                                            \
    LAUNDER(ll);                                                                                                           \
    const u32 cc = ll & 15, gg = ll >> 4;                                                                                  \
    csa = T_CS + wq_ * 256 + gg * 16; kva = L_M + wq_ * 2048 + ll * 16; csv_a = T_CS + wq_ * 256 + 128 + cc * 4;           \
    tb = L_MISC1 + wh * 128 + cc * 4; tv = L_MISC1 + wh * 128 + gg * 16;                                                   \
    tk0 = tv + (cur0 ? 64 : 0) + (cc & 1) * 4; tk1 = tv + (cur1 ? 72 : 8) + (cc & 1) * 4;                                  \
    LAUNDER(csa); LAUNDER(kva); LAUNDER(csv_a); LAUNDER(tb); LAUNDER(tk0); LAUNDER(tk1); LAUNDER(tv);                      \
  }
  {
    u32 xa[2][2], wa[2];
    xa[0][0] = (2 * wh) * 2048 + c * 128 + ((g ^ (c & 7)) << 4);
    xa[0][1] = xa[0][0] ^ 64;
    wa[0] = 10240 + g * 3072 + q4 * 128 + (trl ^ (wq_ << 5));
    xa[1][0] = xa[0][0] + 2 * STG; xa[1][1] = xa[0][1] + 2 * STG; wa[1] = wa[0] + 2 * STG;
    LAUNDER(xa[0][0]); LAUNDER(xa[0][1]); LAUNDER(wa[0]);
    LAUNDER(xa[1][0]); LAUNDER(xa[1][1]); LAUNDER(wa[1]);
    u32 tb, tk0, tk1, tv;
    u32 vgs = WS_SX1 + (R * 64 + 8 * C + w) * 16;
    LAUNDER(vgs);
#define BIAS()                                                                                                                \
  {                                                                                                                           \
    const v4u rb1 = mkrs(a.b1, F_ * 2), rb2 = mkrs(a.b2, S_ * 2);                                                             \
    u32 ll = l;                                                                                                               \
    LAUNDER(ll);                                                                                                              \
    asm volatile("buffer_load_dwordx2 %0, %1, %2, %3 offen" : "=v"(b1v) : "v"((ll >> 4) * 8), "s"(rb1), "s"((FT * 64 + 16 * wq_) * 2) : "memory"); \
    asm volatile("buffer_load_ushort %0, %1, %2, %3 offen" : "=v"(b2v) : "v"((ll & 15) * 2), "s"(rb2), "s"((R * 64 + 16 * wq_) * 2) : "memory");   \
  }
    v4u fx[2][3], fw[2][3];
#define HI(s) ((((s) >> 1) >> 1) & 1)
#define BS(s) ((((s) >> 1) & 1) * STG)
#define RW(s, i) fw[(s)&1][i] = trfrag(wa[HI(s)] + BS(s) + ((s)&1) * 12288 + (i)*1024)
#define RX(s, i) fx[(s)&1][i] = lds128(xa[HI(s)][(s)&1] + BS(s) + (i)*2048)
#define TAB1() { rq1 = *(LAS float*)(tb + 64); rq2 = *(LAS float*)(tb + 128); rka.x = *(LAS float*)(tk0); rkb.x = *(LAS float*)(tk0 + 64); rka.y = *(LAS float*)(tk1); rkb.y = *(LAS float*)(tk1 + 64); }
#define TAB2() { ml0 = *(LAS float*)(tb + 512); ml1 = *(LAS float*)(tb + 512 + 64); ml2 = *(LAS float*)(tb + 512 + 128); }
#define TAB3() { mv0 = lds128f(tv + 512); mv1 = lds128f(tv + 512 + 64); mv2 = lds128f(tv + 512 + 128); }
#define TAB4() { const float kk = 0.25f * 1.44269504088896f; rq1 *= kk; rq2 *= kk; fa0 = rq1 * rka.x; fa1 = rq1 * rka.y; fb0 = rq2 * rkb.x; fb1 = rq2 * rkb.y; \
                 LAUNDER(fa0); LAUNDER(fa1); LAUNDER(fb0); LAUNDER(fb1); }
#define xres_issue() dma1(rx, vxa, (R * 64 + 8 * w) * 1024 + C * 128, mw1k, L_XRES)
#define STATS(n)                                                                                                              \
  {                                                                                                                           \
    WAITV1(n, xr);                                                                                                            \
    const f2 pv = bc2(__uint_as_float(__builtin_amdgcn_readfirstlane(xr.x << 16)));                                           \
    const f2 d0 = f2{blo(xr.x), bhi(xr.x)} - pv, d1 = f2{blo(xr.y), bhi(xr.y)} - pv, d2 = f2{blo(xr.z), bhi(xr.z)} - pv, d3 = f2{blo(xr.w), bhi(xr.w)} - pv; \
    const f2 sv = (d0 + d1) + (d2 + d3), qv = fma2(d0, d0, d1 * d1) + fma2(d2, d2, d3 * d3);                                  \
    float s1 = sv.x + sv.y, s2 = qv.x + qv.y;                                                                                 \
    wsum2(s1, s2);                                                                                                            \
    const float md = s1 * (1.f / 512.f), var = fmx(s2 * (1.f / 512.f) - md * md, 0.f);                                        \
    const v4u gr = {__float_as_uint(__builtin_amdgcn_rsqf(var + 1e-5f)), tag, __float_as_uint(pv.x + md), tag};               \
    ST128M(rws, vgs, gr, 1);                                                                                                  \
  }
#define P1A(s, BARX, D1, D2)                                                      \
  {                                                                               \
    TS(s)                                                                         \
    aQ1 = mfma32(fw[(s)&1][0], fx[(s)&1][1], aQ1);                                \
    aQ2 = mfma32(fw[(s)&1][0], fx[(s)&1][2], aQ2);                                \
    aK1 = mfma32(fw[(s)&1][1], fx[(s)&1][1], aK1); SB();                          \
    BARX; SB();                                                                   \
    RW((s) + 1, 0); RX((s) + 1, 1); SB();                                         \
    aK2 = mfma32(fw[(s)&1][1], fx[(s)&1][2], aK2); SB();                          \
    RX((s) + 1, 2); SB();                                                         \
    aV1 = mfma32(fx[(s)&1][1], fw[(s)&1][2], aV1); SB();                          \
    RW((s) + 1, 1); SB();                                                         \
    aV2 = mfma32(fx[(s)&1][2], fw[(s)&1][2], aV2); SB();                          \
    RW((s) + 1, 2); SB();                                                         \
    aQ0 = mfma32(fw[(s)&1][0], fx[(s)&1][0], aQ0); SB();                          \
    RX((s) + 1, 0); SB();                                                         \
    D1; SB();                                                                     \
    aK0 = mfma32(fw[(s)&1][1], fx[(s)&1][0], aK0); SB();                          \
    D2; SB();                                                                     \
    aV0 = mfma32(fx[(s)&1][0], fw[(s)&1][2], aV0); SB();                          \
  }
#define P1B(s, BARX, D1, D2, D3)                                                  \
  {                                                                               \
    TS(s)                                                                         \
    aQ1 = mfma32(fw[(s)&1][0], fx[(s)&1][1], aQ1);                                \
    aQ2 = mfma32(fw[(s)&1][0], fx[(s)&1][2], aQ2); SB();                          \
    BARX; SB();                                                                   \
    RW((s) + 1, 0); RX((s) + 1, 1); RX((s) + 1, 2); SB();                         \
    D1; SB();                                                                     \
    aK1 = mfma32(fw[(s)&1][1], fx[(s)&1][1], aK1); SB();                          \
    RW((s) + 1, 1); SB();                                                         \
    D2; SB();                                                                     \
    aK2 = mfma32(fw[(s)&1][1], fx[(s)&1][2], aK2); SB();                          \
    RW((s) + 1, 2); SB();                                                         \
    aV1 = mfma32(fx[(s)&1][1], fw[(s)&1][2], aV1); SB();                          \
    D3; SB();                                                                     \
    aV2 = mfma32(fx[(s)&1][2], fw[(s)&1][2], aV2); SB();                          \
  }
#define PBAR(n) { WAITV(n); BAR(); }
    asm volatile("s_waitcnt lgkmcnt(0)" : "+s"(tag)::"memory");
    tag += 0x9E3779B9u;
    STATS(3)
    stage_w(1);
    if (wh == 0) {
      stage_w(2);
      STAMP(22);
      PBAR(1 + 6)
      STAMP(1);
      RW(0, 0); RX(0, 1); RX(0, 2); RW(0, 1); RW(0, 2); RX(0, 0); SB();
      P1A(0, , stage_w(3), )
      P1A(1, PBAR(6), stage_w1(4, 0), stage_w1(4, 1)) P1A(2, , stage_w1(4, 2), PRE_W())
      P1A(3, PBAR(6), stage_w1(5, 0), stage_w1(5, 1)) P1A(4, , stage_w1(5, 2), PRE_K())
      P1A(5, PBAR(6), stage_w1(6, 0), stage_w1(6, 1)) P1A(6, , stage_w1(6, 2), PRE_ST())
      STAMP(16);
      P1A(7, PBAR(6), stage_w1(7, 0), stage_w1(7, 1)) P1A(8, , stage_w1(7, 2), PRE_TAB())
      P1A(9, PBAR(6), wo_issue(0, 0), PRE_SEL()) P1A(10, , wo_issue(0, 1), )
      P1A(11, PBAR(3 + 4), wo_issue(1, 0), TAB1()) P1A(12, , wo_issue(1, 1), TAB2())
      P1A(13, PBAR(8), xres_issue(), TAB4()) P1A(14, , TAB3(), BIAS())
      TS(15)
      aK2 = mfma32(fw[1][1], fx[1][2], aK2);
      aV2 = mfma32(fx[1][2], fw[1][2], aV2);
      aQ0 = mfma32(fw[1][0], fx[1][0], aQ0);
      aK0 = mfma32(fw[1][1], fx[1][0], aK0);
      aV0 = mfma32(fx[1][0], fw[1][2], aV0); SB();
      *(LAS f4*)(kva) = aK2;
      aQ1 = mfma32(fw[1][0], fx[1][1], aQ1); SB();
      *(LAS f4*)(kva + 1024) = aV2;
      aK1 = mfma32(fw[1][1], fx[1][1], aK1);
      aQ2 = mfma32(fw[1][0], fx[1][2], aQ2);
      aV1 = mfma32(fx[1][1], fw[1][2], aV1);
      LAUNDER(aQ1); LAUNDER(aQ2); LAUNDER(aK1); LAUNDER(aV1); SB();
      asm volatile("s_nop 1\n\ts_mov_b64 exec, %4\n\tds_write_b128 %0, %1\n\tds_write_b128 %0, %2 offset:64\n\ts_mov_b64 exec, 0xffff\n\tds_write_b32 %5, %3\n\ts_mov_b64 exec, -1"
                   ::"v"(csa), "v"(aQ0), "v"(aK0), "v"(aV0[0]), "s"(0x0001000100010001ull), "v"(csv_a) : "memory");
      TS(16)
    } else {
      v4u gst;
      if (PRIO) __builtin_amdgcn_s_setprio(PRIO);
      stage_x(1); stage_w(2); stage_x(2);
      STAMP(22);
      PBAR(1 + 12)
      STAMP(1);
      RW(0, 0); RX(0, 1); RX(0, 2); RW(0, 1); RW(0, 2); SB();
      P1B(0, , stage_w(3), { stage_xa(3); stage_xb(3); }, stage_xh(3))
#define P1BC(s, ch, n) P1B(s, PBAR(n), stage_xa(ch), stage_xb(ch), stage_xh(ch)) P1B((s) + 1, , stage_w1(ch, 0), stage_w1(ch, 1), stage_w1(ch, 2))
      P1BC(1, 4, 12)
      int lq = l;
      LAUNDER(lq);
      lq = lq < 20 ? lq : 0;
      u32 gvo = WS_SX1 + (u32)max(r0 + wq_ * 20 + lq, 0) * 16, gta = L_MISC1 + wq_ * 80 + lq * 4;
      LAUNDER(gvo); LAUNDER(gta);
      P1BC(3, 5, 12)
      LD_SC1(gst, rws, gvo, 0, 0);
      PRE_W()
      P1BC(5, 6, 12 + 1)
      STAMP(16);
      PRE_K()
      P1BC(7, 7, 12 + 1)
#define GCHECK()                                                                                                                      \
  {                                                                                                                                   \
    LAUNDER(gst);                                                                                                                     \
    if (__builtin_expect(!__all(gst.y == tag && gst.w == tag), 0)) {                                                                  \
      _Pragma("nounroll") for (u32 sp = 0; sp < SPIN_CAP; ++sp) {                                                                     \
        asm volatile("buffer_load_dwordx4 %0, %1, %2, 0 offen sc1\n\ts_waitcnt vmcnt(0)" : "=v"(gst) : "v"(gvo), "s"(rws) : "memory"); \
        if (__all(gst.y == tag && gst.w == tag)) break;                                                                               \
      }                                                                                                                               \
    }                                                                                                                                 \
    *(LAS u32*)(gta) = gst.x;                                                                                                         \
    *(LAS u32*)(gta + 512) = gst.z;                                                                                                   \
  }
      P1B(9, PBAR(12), PRE_ST(), wo_issue(0, 0), GCHECK()) P1B(10, , PRE_TAB(), wo_issue(0, 1), PRE_SEL())
      P1B(11, PBAR(6 + 4), , wo_issue(1, 0), TAB1()) P1B(12, , , wo_issue(1, 1), TAB2())
      P1B(13, PBAR(8), , xres_issue(), TAB4()) P1B(14, , , BIAS(), TAB3())
      TS(15)
      aQ1 = mfma32(fw[1][0], fx[1][1], aQ1);
      aK1 = mfma32(fw[1][1], fx[1][1], aK1);
      aQ2 = mfma32(fw[1][0], fx[1][2], aQ2);
      aK2 = mfma32(fw[1][1], fx[1][2], aK2);
      aV1 = mfma32(fx[1][1], fw[1][2], aV1);
      aV2 = mfma32(fx[1][2], fw[1][2], aV2);
      LAUNDER(aQ1); LAUNDER(aQ2); LAUNDER(aK1); LAUNDER(aK2); LAUNDER(aV1); LAUNDER(aV2);
      if (PRIO) __builtin_amdgcn_s_setprio(0);
      TS(16)
    }
  }
  BAR();
  TS(17)
  STAMP(2);
  {
    const f4 csq = lds128f(csa), csk = lds128f(csa + 64);
    const float csv = *(LAS float*)(csv_a);
    if (wh == 1) {
      aK0 = lds128f(kva);
      aV0 = lds128f(kva + 1024);
    }
    auto foldT = [&](f4 acc, float m, f4 cs) {
      v2u r;
      r.x = pk2(fma2(bc2(-m), lo2(cs), lo2(acc)));
      r.y = pk2(fma2(bc2(-m), hi2(cs), hi2(acc)));
      return r;
    };
    auto foldV = [&](f4 acc, f4 m) {
      v2u r;
      r.x = pk2(fma2(lo2(m), bc2(-csv), lo2(acc)));
      r.y = pk2(fma2(hi2(m), bc2(-csv), hi2(acc)));
      return r;
    };
    const f4 z4 = {0, 0, 0, 0};
    const v2u Q1 = foldT(aQ1, ml1, csq), Q2 = foldT(aQ2, ml2, csq), K1 = foldT(aK1, ml1, csk), K2 = foldT(aK2, ml2, csk);
    SB();
    const f4 sbp = mfma16(K1, Q2, z4), sac = mfma16(K1, Q1, z4), sbc = mfma16(K2, Q2, z4);
    const v2u K0 = foldT(aK0, ml0, csk);
    SB();
    const v2u V1 = foldV(aV1, mv1);
    const f4 sap = mfma16(K0, Q1, z4);
    const v2u V2 = foldV(aV2, mv2), V0 = foldV(aV0, mv0);
    SB();
    const float b0 = (cur0 ? (odd ? sbc[1] : sbc[0]) : (odd ? sbp[1] : sbp[0])) * fb0, b1 = (cur1 ? (odd ? sbc[3] : sbc[2]) : (odd ? sbp[3] : sbp[2])) * fb1;
    const float a0 = fmaf(cur0 ? (odd ? sac[1] : sac[0]) : (odd ? sap[1] : sap[0]), fa0, na0), a1 = fmaf(cur1 ? (odd ? sac[3] : sac[2]) : (odd ? sap[3] : sap[2]), fa1, na1);
    auto m1 = __builtin_amdgcn_permlane16_swap(__float_as_uint(fmx(a0, a1)), __float_as_uint(fmx(b0, b1)), false, false);
    const float m2 = sw32max(fmx(__uint_as_float(m1[0]), __uint_as_float(m1[1])));
    auto m3 = __builtin_amdgcn_permlane16_swap(__float_as_uint(m2), __float_as_uint(m2), false, false);
    const float ma = __uint_as_float(m3[0]), mb = __uint_as_float(m3[1]);
    const f2 ea = {__builtin_amdgcn_exp2f(a0 - ma), __builtin_amdgcn_exp2f(a1 - ma)}, eb = {__builtin_amdgcn_exp2f(b0 - mb), __builtin_amdgcn_exp2f(b1 - mb)};
    const u32 pa = pk2(ea * rka), pb = pk2(eb * rkb);
    const v4u Pa = {__builtin_amdgcn_perm(0, pa, sel0), __builtin_amdgcn_perm(0, pa, sel1), __builtin_amdgcn_perm(0, pa, sel2), __builtin_amdgcn_perm(0, pa, sel3)};
    const v4u Pb = {__builtin_amdgcn_perm(0, pb, sel0), __builtin_amdgcn_perm(0, pb, sel1), __builtin_amdgcn_perm(0, pb, sel2), __builtin_amdgcn_perm(0, pb, sel3)};
    const v4u Va = {V0.x, V0.y, V1.x, V1.y}, Vb = {V1.x, V1.y, V2.x, V2.y};
    const f4 oa4 = mfma32(Va, Pa, z4), ob4 = mfma32(Vb, Pb, z4);
    SB();
    auto s1 = __builtin_amdgcn_permlane16_swap(__float_as_uint(ea.x + ea.y), __float_as_uint(eb.x + eb.y), false, false);
    const f2 inv = bc2(__builtin_amdgcn_rcpf(sw32add(__uint_as_float(s1[0]) + __uint_as_float(s1[1]))));
    SB();
    f4 x4, y4;
#pragma unroll
    for (int i = 0; i < 4; ++i) {
      auto t = __builtin_amdgcn_permlane16_swap(__float_as_uint(oa4[i]), __float_as_uint(ob4[i]), false, false);
      x4[i] = __uint_as_float(t[0]);
      y4[i] = __uint_as_float(t[1]);
    }
    const v4u ov = {pk2(lo2(x4) * inv), pk2(hi2(x4) * inv), pk2(lo2(y4) * inv), pk2(hi2(y4) * inv)};
    st_sc1_128(rws, st_o, ov);
  }
  TS(18)
  TSFIN(19)
  STAMP(3);
  if (p3) {
    w1_issue(0, 4); w1_issue(1, 5); w1_issue(2, 6);
    WAITV(6);
  } else {
    WAITV(0);
  }
  asm volatile("" : "+v"(b1v), "+v"(b2v));
  {
    u32 fo = WS_FLAGA + (R * 8 + w) * 128 + C * 4, tv_ = tag;
    LAUNDER(fo); LAUNDER(tv_);
    BAR();
    ST32M(rws, fo, tv_, 1);
  }
  STAMP(4);
  f4 h0, h1;
  v4u bw[6][4];
  const u32 trb = wq_ * 4096 + (8 * g + q4) * 128 + (trl ^ (wq_ << 5));
#define TRSET(grp) { u32 tg_ = trb + (grp)*65536; LAUNDER(tg_); tr[0] = tg_; tr[1] = tg_ ^ 32; tr[2] = tg_ ^ 64; tr[3] = tg_ ^ 96; }
#define BREAD(dst, slot) _Pragma("unroll") for (int t = 0; t < 4; ++t) dst[t] = trfrag(tr[t] + ((slot)&3) * 16384);
  u32 tr[4];
  {
    if (p3 && wh == 1) w1_issue4(3, 7);
    v4u wf[8][4];
    {
      u32 t0 = wh * 32768 + (8 * g + q4) * 128 + (trl ^ (wh << 6));
      LAUNDER(t0);
#pragma unroll
      for (int kk = 0; kk < 8; ++kk)
#pragma unroll
        for (int t = 0; t < 4; ++t) wf[kk][t] = trfrag((t0 ^ (t << 5)) + kk * 4096);
    }
    f4 acc[4] = {{0, 0, 0, 0}, {0, 0, 0, 0}, {0, 0, 0, 0}, {0, 0, 0, 0}};
    {
      const u32 ra = L_XRES + (16 * wq_ + c) * 128 + (((4 * wh + (g >> 1)) ^ (c & 7)) << 4) + 8 * (g & 1);
      const v2u xres0 = *(LAS v2u*)(ra), xres1 = *(LAS v2u*)(ra ^ 32);
      acc[0] = f4{blo(xres0.x), bhi(xres0.x), blo(xres0.y), bhi(xres0.y)};
      acc[1] = f4{blo(xres1.x), bhi(xres1.x), blo(xres1.y), bhi(xres1.y)};
      LAUNDER(acc[0]); LAUNDER(acc[1]);
    }
    const u32 own = L_M + (w ^ 4) * 2048;
    u32 rdw = L_M + w * 2048 + l * 16, rdr = own + l * 16, pwa = own + 1024 + l * 8, pra = L_M + g * 2048 + 1024 + c * 8, rca = L_M + 4 * 2048 + 1536 + (16 * wq_ + c) * 8;
    u32 twa = own + c * 64 + g * 8, tra = own + (8 * (g >> 1) + q4) * 64 + (g & 1) * 32 + p4 * 8, gso = WS_SX + (R * 8 + C) * 1024 + l * 16;
    LAUNDER(rdw); LAUNDER(rdr); LAUNDER(pwa); LAUNDER(pra); LAUNDER(rca); LAUNDER(twa); LAUNDER(tra); LAUNDER(gso);
    u32 so = WS_OF + ((4 * R + wq_) * 16 + 8 * wh) * 1024, so2 = so + 4096;
    asm volatile("" : "+s"(so), "+s"(so2));
    if (w == 0) {
      const u32 vo = WS_FLAGA + (R * 8 + C) * 128 + (l & 7) * 4;
      POLL(vo)
    }
    BAR();
    STAMP(5);
    v4u of[8];
    LD_SC1(of[0], rws, avo, so, 0); LD_SC1(of[1], rws, avo, so, 1024); LD_SC1(of[2], rws, avo, so, 2048); LD_SC1(of[3], rws, avo, so, 3072);
    LD_SC1(of[4], rws, avo, so2, 0); LD_SC1(of[5], rws, avo, so2, 1024); LD_SC1(of[6], rws, avo, so2, 2048); LD_SC1(of[7], rws, avo, so2, 3072);
    SB();
#define P2STEP(kk) WAITV1(7 - kk, of[kk]); acc[2] = mfma32(wf[kk][2], of[kk], acc[2]); acc[3] = mfma32(wf[kk][3], of[kk], acc[3]); acc[0] = mfma32(wf[kk][0], of[kk], acc[0]); acc[1] = mfma32(wf[kk][1], of[kk], acc[1]); SB();
    P2STEP(0) P2STEP(1) P2STEP(2) P2STEP(3) P2STEP(4) P2STEP(5) P2STEP(6) P2STEP(7)
    STAMP(6);
    *(LAS f4*)(rdw) = acc[2];
    *(LAS f4*)(rdw + 1024) = acc[3];
    BAR();
    h0 = acc[0] + lds128f(rdr);
    h1 = acc[1] + lds128f(rdr + 1024);
    {
      const f2 sv = (lo2(h0) + hi2(h0)) + (lo2(h1) + hi2(h1)), qv = fma2(lo2(h0), lo2(h0), hi2(h0) * hi2(h0)) + fma2(lo2(h1), lo2(h1), hi2(h1) * hi2(h1));
      float s1 = sv.x + sv.y, s2 = qv.x + qv.y;
      LAUNDER(s1); LAUNDER(s2);
      *(LAS f2*)(pwa) = f2{s1, s2};
    }
    BAR();
    STAMP(7);
    if (w == 0) {
      f2 t[8];
#pragma unroll
      for (int i = 0; i < 4; ++i) { t[i] = *(LAS f2*)(pra + i * 128); t[4 + i] = *(LAS f2*)(pra + 8192 + i * 128); }
      SB();
      const f2 tt = ((t[0] + t[4]) + (t[1] + t[5])) + ((t[2] + t[6]) + (t[3] + t[7]));
      const v4u gr = {__float_as_uint(tt.x), tag, __float_as_uint(tt.y), tag};
      st_sc1_128(rws, gso, gr);
    }
    if (p3) {
      if (wh == 1) { w1_issue4(4, 8); w1_issue4(5, 0); w1_issue4(6, 1); w1_issue4(7, 2); w1_issue4(8, 3); }
      TRSET(1) BREAD(bw[0], 4) BREAD(bw[1], 5) BREAD(bw[2], 6) BREAD(bw[3], 7)
    }
    if (w == 0) {
      WAITV(0);
      v4u gg[8];
      const u32 sxb = WS_SX + R * 8192;
      _Pragma("nounroll") for (u32 sp = 0; sp < SPIN_CAP; ++sp) {
        asm volatile(
            "buffer_load_dwordx4 %0, %8, %9, %10 offen sc1\n\tbuffer_load_dwordx4 %1, %8, %9, %10 offen offset:1024 sc1\n\t"
            "buffer_load_dwordx4 %2, %8, %9, %10 offen offset:2048 sc1\n\tbuffer_load_dwordx4 %3, %8, %9, %10 offen offset:3072 sc1\n\t"
            "buffer_load_dwordx4 %4, %8, %9, %11 offen sc1\n\tbuffer_load_dwordx4 %5, %8, %9, %11 offen offset:1024 sc1\n\t"
            "buffer_load_dwordx4 %6, %8, %9, %11 offen offset:2048 sc1\n\tbuffer_load_dwordx4 %7, %8, %9, %11 offen offset:3072 sc1\n\ts_waitcnt vmcnt(0)"
            : "=&v"(gg[0]), "=&v"(gg[1]), "=&v"(gg[2]), "=&v"(gg[3]), "=&v"(gg[4]), "=&v"(gg[5]), "=&v"(gg[6]), "=&v"(gg[7])
            : "v"(avo), "s"(rws), "s"(sxb), "s"(sxb + 4096)
            : "memory");
        u32 d = 0;
#pragma unroll
        for (int i = 0; i < 8; ++i) d |= (gg[i].y ^ tag) | (gg[i].w ^ tag);
        if (__all(d == 0)) break;
      }
      auto F = [](u32 v) { return __uint_as_float(v); };
      const float t1 = ((F(gg[0].x) + F(gg[1].x)) + (F(gg[2].x) + F(gg[3].x))) + ((F(gg[4].x) + F(gg[5].x)) + (F(gg[6].x) + F(gg[7].x)));
      const float t2 = ((F(gg[0].z) + F(gg[1].z)) + (F(gg[2].z) + F(gg[3].z))) + ((F(gg[4].z) + F(gg[5].z)) + (F(gg[6].z) + F(gg[7].z)));
      const float m = t1 * (1.f / 512.f);
      const float var = fmx(t2 * (1.f / 512.f) - m * m, 0.f);
      const float r1 = __builtin_amdgcn_rsqf(var + 1e-5f);
      const float r = r1 * __builtin_amdgcn_rsqf(var * r1 * r1 + 1e-5f);
      *(LAS f2*)(pwa + 512) = f2{r, -m * r};
    }
    BAR();
    STAMP(8);
    const f2 rc = *(LAS f2*)(rca);
    const f2 rr = bc2(rc.x), cc = bc2(rc.y);
    const v2u n0 = {pk2(fma2(lo2(h0), rr, cc)), pk2(fma2(hi2(h0), rr, cc))}, n1 = {pk2(fma2(lo2(h1), rr, cc)), pk2(fma2(hi2(h1), rr, cc))};
    *(LAS v2u*)(twa) = n0;
    *(LAS v2u*)(twa + 32) = n1;
    asm volatile("" ::: "memory");
    const v2u y0 = trd(tra), y1 = trd(tra + 256);
    const v4u hv = {y0.x, y0.y, y1.x, y1.y};
    st_sc1_128(rws, st_h, hv);
  }
  {
    const float bs = blo(b2v);
    h0 += bs;
    h1 += bs;
  }
  f4 acc[4][2];
  v4u bf[2][4];
  u32 rbw = 7 * 16384 + w * 6144 + l * 16, rb1_ = 7 * 16384 + (w ^ 1) * 6144 + l * 16, rb2_ = 7 * 16384 + (w ^ 2) * 6144 + 2048 + l * 16, rb3_ = 7 * 16384 + (w ^ 3) * 6144 + 4096 + l * 16;
  LAUNDER(rbw); LAUNDER(rb1_); LAUNDER(rb2_); LAUNDER(rb3_);
#define GSTEP(MM, n, NW, B, RD, NB, slot, D1, D2)                                 \
  {                                                                               \
    SB();                                                                         \
    WAITV2(NW, af[n][0], af[n][1]);                                               \
    MM(0, 0, n, B); SB();                                                         \
    if (RD) { NB[0] = trfrag(tr[0] + ((slot)&3) * 16384); } SB();                 \
    MM(0, 1, n, B); SB();                                                         \
    if (RD) { NB[1] = trfrag(tr[1] + ((slot)&3) * 16384); } SB();                 \
    MM(1, 0, n, B); SB();                                                         \
    if (RD) { NB[2] = trfrag(tr[2] + ((slot)&3) * 16384); } SB();                 \
    MM(1, 1, n, B); SB();                                                         \
    if (RD) { NB[3] = trfrag(tr[3] + ((slot)&3) * 16384); } SB();                 \
    MM(2, 0, n, B); SB();                                                         \
    D1; SB();                                                                     \
    MM(2, 1, n, B);                                                               \
    MM(3, 0, n, B); SB();                                                         \
    D2; SB();                                                                     \
    MM(3, 1, n, B); SB();                                                         \
  }
#define GLAST(MM, n, NW, B, r0_, r1_)                                             \
  {                                                                               \
    SB();                                                                         \
    WAITV2(NW, af[n][0], af[n][1]);                                               \
    MM(1, 0, n, B); MM(1, 1, n, B); MM(2, 0, n, B); MM(2, 1, n, B); SB();         \
    *(LAS f4*)(rbw) = acc[1][0]; SB();                                            \
    MM(3, 0, n, B); SB();                                                         \
    *(LAS f4*)(rbw + 1024) = acc[1][1]; SB();                                     \
    MM(3, 1, n, B); SB();                                                         \
    *(LAS f4*)(rbw + 2048) = acc[2][0]; SB();                                     \
    MM(0, 0, n, B); SB();                                                         \
    *(LAS f4*)(rbw + 3072) = acc[2][1]; SB();                                     \
    MM(0, 1, n, B); SB();                                                         \
    *(LAS f4*)(rbw + 4096) = acc[3][0];                                           \
    *(LAS f4*)(rbw + 5120) = acc[3][1];                                           \
    LAUNDER(acc[0][0]); LAUNDER(acc[0][1]);                                       \
    BAR();                                                                        \
    const f4 p0 = lds128f(rb1_), p2 = lds128f(rb2_), p4_ = lds128f(rb3_), p1 = lds128f(rb1_ + 1024), p3_ = lds128f(rb2_ + 1024), p5 = lds128f(rb3_ + 1024); \
    SB();                                                                         \
    r0_ = (acc[0][0] + p0) + (p2 + p4_);                                          \
    r1_ = (acc[0][1] + p1) + (p3_ + p5);                                          \
  }
  if (p3) {
    w1_issue(9, 4); w1_issue(10, 5); w1_issue(11, 6);
    WAITV(6);
    {
      u32 fo = WS_FLAGC + ((4 * w + l) * 8 + C) * 128 + R * 4, tv_ = tag;
      LAUNDER(fo); LAUNDER(tv_);
      BAR();
      ST32M(rws, fo, tv_, 15);
    }
    STAMP(9);
    if (wh == 1) { w1_issue4(12, 7); w1_issue4(13, 9); }
    TRSET(2) BREAD(bw[4], 8)
    TRSET(0) BREAD(bw[5], 0)
    u32 abase = WS_HF + ((4 * C + 2 * wh) * 64 + 16 * wq_) * 1024;
    asm volatile("" : "+s"(abase));
    acc[0][0] = acc[0][1] = f4{blo(b1v.x), bhi(b1v.x), blo(b1v.y), bhi(b1v.y)};
    LAUNDER(acc[0][0]); LAUNDER(acc[0][1]);
#pragma unroll
    for (int t = 1; t < 4; ++t) acc[t][0] = acc[t][1] = f4{0, 0, 0, 0};
    if (w == 0) {
      const u32 vo = WS_FLAGC + (FT * 8 + C) * 128 + (l & 31) * 4;
      POLL(vo)
    }
    BAR();
    STAMP(10);
#define J3(n) ((n) < 2 ? (n) : ((n) <= 11 ? (n) + 4 : (n)-10))
    v4u af[16][2];
#define A3L(n, m) { const u32 so = abase + KS3(J3(n)) * 1024 + (m)*65536; LD_SC1(af[n][m], rws, avo, so, 0); }
    A3L(0, 0) A3L(0, 1) A3L(1, 0) A3L(1, 1) A3L(2, 0) A3L(2, 1) A3L(3, 0) A3L(3, 1)
#define MM3(t, m, n, B) acc[t][m] = mfma32(B[t], af[n][m], acc[t][m])
    GSTEP(MM3, 0, 6, bw[0], 0, bf[0], 0, A3L(4, 0), A3L(4, 1))
    BAR();
    STAMP(17);
    GSTEP(MM3, 1, 6, bw[1], 1, bf[0], 1, { A3L(5, 0) A3L(5, 1) }, w1_issue(14, 8))
    GSTEP(MM3, 2, 6 + 2, bf[0], 1, bf[1], 2, { A3L(6, 0) A3L(6, 1) }, w1_issue(15, 0))
    GSTEP(MM3, 3, 6 + 4, bf[1], 1, bf[0], 3, A3L(7, 0), A3L(7, 1))
    TRSET(1)
    GSTEP(MM3, 4, 6 + 4, bf[0], 1, bf[1], 4, A3L(8, 0), A3L(8, 1))
    GSTEP(MM3, 5, 6 + 4, bf[1], 1, bf[0], 5, A3L(9, 0), A3L(9, 1))
    GSTEP(MM3, 6, 6 + 2, bf[0], 1, bf[1], 6, A3L(10, 0), A3L(10, 1))
    GSTEP(MM3, 7, 6, bf[1], 1, bf[0], 7, A3L(11, 0), A3L(11, 1))
    TRSET(2)
    GSTEP(MM3, 8, 6, bf[0], 1, bf[1], 9, A3L(12, 0), A3L(12, 1))
    BAR();
    GSTEP(MM3, 9, 6, bf[1], 1, bf[0], 8, A3L(13, 0), A3L(13, 1))
    TRSET(0)
    GSTEP(MM3, 10, 6, bf[0], 1, bf[1], 0, A3L(14, 0), A3L(14, 1))
    GSTEP(MM3, 11, 6, bf[1], 0, bf[0], 0, A3L(15, 0), A3L(15, 1))
    BAR();
    STAMP(18);
    GSTEP(MM3, 12, 6, bw[2], 0, bf[1], 0, w2_issue(0, 0), w2_issue(1, 1))
    GSTEP(MM3, 13, 4 + 4, bw[3], 0, bf[1], 0, w2_issue(2, 2), )
    GSTEP(MM3, 14, 2 + 6, bw[4], 0, bf[1], 0, w2_issue(3, 3), )
    STAMP(11);
    f4 u0, u1;
    GLAST(MM3, 15, 0 + 8, bw[5], u0, u1)
    auto gelu2 = [&](f2 v) {
      const float k1 = -2.f * 0.7978845608028654f * 1.44269504088896f, k2 = k1 * 0.044715f;
      const f2 z = v * fma2(v * v, bc2(k2), bc2(k1));
      const f2 d = f2{__builtin_amdgcn_exp2f(z.x), __builtin_amdgcn_exp2f(z.y)} + bc2(1.f);
      return pk2(v * f2{__builtin_amdgcn_rcpf(d.x), __builtin_amdgcn_rcpf(d.y)});
    };
    const v2u ua = {gelu2(lo2(u0)), gelu2(hi2(u0))}, ub = {gelu2(lo2(u1)), gelu2(hi2(u1))};
    st_sc1_128(rws, st_u, widen(ua, ub));
    STAMP(19);
    w2_issue(4, 4); w2_issue(5, 5); w2_issue(6, 6);
    WAITV(6);
    {
      u32 fo = WS_FLAGD + ((4 * w + l) * 8 + C) * 128 + FT * 4, tv_ = tag;
      LAUNDER(fo); LAUNDER(tv_);
      BAR();
      ST32M(rws, fo, tv_, 15);
    }
    if (b == 0 && tid == 128) st_sc1_32(rws, WS_EPOCH, tag);
  } else {
    WAITV(0);
    BAR();
    if (l < 4) st_sc1_32(rws, WS_FLAGC + ((4 * w + l) * 8 + C) * 128 + R * 4, tag);
    w2_issue(0, 0); w2_issue(1, 1); w2_issue(2, 2); w2_issue(3, 3); w2_issue(4, 4); w2_issue(5, 5); w2_issue(6, 6);
    WAITV(0);
    BAR();
  }
  STAMP(12);
  if (wh == 1) { w2_issue4(7, 7); w2_issue4(8, 8); w2_issue4(9, 9); }
  TRSET(0) BREAD(bw[0], 0) BREAD(bw[1], 1) BREAD(bw[2], 2) BREAD(bw[3], 3)
  {
    u32 abase = WS_UF + ((4 * C + 2 * wh) * 56 + 14 * wq_) * 1024;
    asm volatile("" : "+s"(abase));
    u32 oo = (R * 64 + 16 * wq_ + c) * 1024 + (64 * C + 32 * wh + 16 * (g & 1) + 8 * (g >> 1)) * 2;
    LAUNDER(oo);
    acc[0][0] = h0;
    acc[0][1] = h1;
#pragma unroll
    for (int t = 1; t < 4; ++t) acc[t][0] = acc[t][1] = f4{0, 0, 0, 0};
    if (w == 0) {
      const u32 vo = WS_FLAGD + (R * 8 + C) * 128 + (l < 28 ? l : 0) * 4;
      POLL(vo)
    }
    BAR();
    STAMP(13);
    v4u af[14][2];
#define A4L(n, m) { const u32 so = abase + KS4(n) * 1024 + (m)*56 * 1024; LD_SC1(af[n][m], rws, avo, so, 0); }
    A4L(0, 0) A4L(0, 1) A4L(1, 0) A4L(1, 1) A4L(2, 0) A4L(2, 1) A4L(3, 0) A4L(3, 1)
#define MM4(t, m, n, B) acc[t][m] = mfma32(af[n][m], B[t], acc[t][m])
    GSTEP(MM4, 0, 6, bw[0], 0, bf[0], 0, { A4L(4, 0) A4L(4, 1) }, w2_issue(10, 0))
    GSTEP(MM4, 1, 6 + 2, bw[1], 0, bf[0], 0, { A4L(5, 0) A4L(5, 1) }, w2_issue(11, 1))
    GSTEP(MM4, 2, 6 + 4, bw[2], 0, bf[0], 0, { A4L(6, 0) A4L(6, 1) }, w2_issue(12, 2))
    BAR();
    STAMP(20);
    TRSET(1)
    GSTEP(MM4, 3, 6 + 6, bw[3], 1, bf[0], 4, { A4L(7, 0) A4L(7, 1) }, w2_issue(13, 3))
    GSTEP(MM4, 4, 6 + 8, bf[0], 1, bf[1], 5, A4L(8, 0), A4L(8, 1))
    GSTEP(MM4, 5, 6 + 6, bf[1], 1, bf[0], 6, A4L(9, 0), A4L(9, 1))
    GSTEP(MM4, 6, 6 + 4, bf[0], 1, bf[1], 7, A4L(10, 0), A4L(10, 1))
    TRSET(2)
    GSTEP(MM4, 7, 6 + 2, bf[1], 1, bf[0], 8, A4L(11, 0), A4L(11, 1))
    GSTEP(MM4, 8, 6, bf[0], 1, bf[1], 9, A4L(12, 0), A4L(12, 1))
    BAR();
    TRSET(0)
    GSTEP(MM4, 9, 6, bf[1], 1, bf[0], 0, A4L(13, 0), A4L(13, 1))
    GSTEP(MM4, 10, 6, bf[0], 1, bf[1], 1, , )
    GSTEP(MM4, 11, 4, bf[1], 1, bf[0], 2, , )
    GSTEP(MM4, 12, 2, bf[0], 1, bf[1], 3, , )
    STAMP(14);
    f4 y0, y1;
    GLAST(MM4, 13, 0, bf[1], y0, y1)
    const v2u oa = {pk2(lo2(y0)), pk2(hi2(y0))}, ob = {pk2(lo2(y1)), pk2(hi2(y1))};
    *(v4u*)((char*)a.out + oo) = widen(oa, ob);
  }
}

extern "C" void kernel_launch(void* const* d_in, const int* in_sizes, int n_in, void* d_out, int out_size, void* d_ws, size_t ws_size, hipStream_t stream) {
  Args a;
  a.x = d_in[0];
  a.wqkv = d_in[1];
  a.wo = d_in[2];
  a.w1 = d_in[4];
  a.b1 = d_in[5];
  a.w2 = d_in[6];
  a.b2 = d_in[7];
  a.out = d_out;
  a.ws = d_ws;
  (void)hipFuncSetAttribute((const void*)fused_kernel, hipFuncAttributeMaxDynamicSharedMemorySize, LDS_BYTES);
  fused_kernel<<<256, 512, LDS_BYTES, stream>>>(a);
}
```

```cpp
#include <hip/hip_runtime.h>
#include <hip/hip_bf16.h>

typedef unsigned u32;
typedef unsigned long long u64;
typedef u32 v4u __attribute__((ext_vector_type(4)));
typedef u32 v2u __attribute__((ext_vector_type(2)));
typedef u32 v8u __attribute__((ext_vector_type(8)));
typedef float f4 __attribute__((ext_vector_type(4)));
typedef float f2 __attribute__((ext_vector_type(2)));
typedef __bf16 v8bf __attribute__((ext_vector_type(8)));
typedef __bf16 v2bf __attribute__((ext_vector_type(2)));
typedef short v4s __attribute__((ext_vector_type(4)));
#define LAS __attribute__((address_space(3)))
#define DI __device__ __forceinline__

#define XMASK 1
#define XOFF 1
#define PRIO 1
#define AX 0
#define TSTEPA 99
#define TSTEPB 99
#define SWEEP 2
#define BX 0
#define R3 (8 * (bi & 1) + 4 * (bb & 1))
#define NTW 0
#define EARLYW1 2
#define R4 (2 * (bi & 1))
#define BADC 1
#define P1V 0
#define DIST 0
#define PERM3 0xFE7BA36DC5298140ull
#define W2PRE 14
#define NTMASK 0
#define P45 1
#define SW1 0
#define PDLY 0
#define NB1 2
#define NB2 10
#define NE 2
#define NF 4
#define MTR 1
#define PD 3
#define LPRIO 0
#define BST 0
#define ESW 0
#define SW2 1
#define DEF1 1
#define SWLIM 20
#define BX3 ((bi >> 1) & 1)
#define K4MODE 1
#define K4X (cx & 1)
#define NA2 4
#define SP 7
#define SDLY 1
#define NOB 0
#define ABL 0
#define STAMPS 0

constexpr int S_ = 2048, D_ = 512, F_ = 1792;
constexpr u32 WS_EPOCH = 0, WS_FLAGA = 4096, WS_FLAGD = WS_FLAGA + 32768, WS_FLAGC = WS_FLAGD + 32768;
constexpr u32 WS_SX1 = WS_FLAGC + 32768;
constexpr u32 WS_STAMP = 7u << 20, WS_SX = 512 * 1024;
constexpr u32 WS_OF = 1u << 20, WS_HF = 3u << 20, WS_UF = 5u << 20, WS_BYTES = 8u << 20;
constexpr u32 STG = 34816, L_MISC1 = 152 * 1024, L_M = 144 * 1024, LDS_BYTES = 160 * 1024;
constexpr u32 L_CNT = L_MISC1 + 2048;
constexpr u32 SPIN_CAP = 1u << 16;
constexpr u32 CODE_BYTES = 23000;

DI v4u mkrs(const void* p, u32 bytes) {
  v4u r;
  r.x = (u32)(size_t)p;
  r.y = (u32)((size_t)p >> 32) & 0xffffu;
  r.z = bytes;
  r.w = 0x00020000u;
  return r;
}
#define NT2 ""
#define NT3 ""
#define NT1 ""
#define dma1(rs, voff, soff, m0b, imm) \
  asm volatile("s_add_u32 m0, %3, %4\n\ts_nop 0\n\tbuffer_load_dwordx4 %0, %1, %2 offen" NT1 " lds" ::"v"(voff), "s"(rs), "s"(soff), "s"(m0b), "i"(imm) : "memory", "scc")
#define dma2(rs, voffa, voffb, soff, m0b, imm)                                                                                                        \
  asm volatile("s_add_u32 m0, %4, %5\n\ts_nop 0\n\tbuffer_load_dwordx4 %0, %2, %3 offen" NT2 " lds\n\tbuffer_load_dwordx4 %1, %2, %3 offen offset:1024" NT2 " lds" ::"v"(voffa), \
               "v"(voffb), "s"(rs), "s"(soff), "s"(m0b), "i"(imm)                                                                                     \
               : "memory", "scc")
#define dma2nt(rs, voffa, voffb, soff, m0b, imm)                                                                                                        \
  asm volatile("s_add_u32 m0, %4, %5\n\ts_nop 0\n\tbuffer_load_dwordx4 %0, %2, %3 offen nt lds\n\tbuffer_load_dwordx4 %1, %2, %3 offen offset:1024 nt lds" ::"v"(voffa), \
               "v"(voffb), "s"(rs), "s"(soff), "s"(m0b), "i"(imm)                                                                                     \
               : "memory", "scc")
#define dma2p(j, rs, voffa, voffb, soff, m0b, imm) { if ((NTMASK >> (j)) & 1) dma2nt(rs, voffa, voffb, soff, m0b, imm); else dma2(rs, voffa, voffb, soff, m0b, imm); }
#define dma3(rs, voff, soff, m0b, imm)                                                                                                               \
  asm volatile(                                                                                                                                      \
      "s_add_u32 m0, %3, %4\n\ts_nop 0\n\tbuffer_load_dwordx4 %0, %1, %2 offen" NT3 " lds\n\tbuffer_load_dwordx4 %0, %1, %2 offen offset:1024" NT3 " lds\n\tbuffer_load_dwordx4 %0, %1, %2 offen offset:2048" NT3 " lds" ::"v"(voff), \
      "s"(rs), "s"(soff), "s"(m0b), "i"(imm)                                                                                                         \
      : "memory", "scc")
#define dma1o(rs, voff, soff, m0b, imm, off) \
  asm volatile("s_add_u32 m0, %3, %4\n\ts_nop 0\n\tbuffer_load_dwordx4 %0, %1, %2 offen offset:%5" NT3 " lds" ::"v"(voff), "s"(rs), "s"(soff), "s"(m0b), "i"(imm), "i"(off) : "memory", "scc")
#define dma1m(rs, voff, soff, m0b, imm, mask)                                                                                                   \
  asm volatile("s_add_u32 m0, %3, %4\n\ts_mov_b64 exec, %5\n\tbuffer_load_dwordx4 %0, %1, %2 offen" NT1 " lds\n\ts_mov_b64 exec, -1" ::"v"(voff), "s"(rs), \
               "s"(soff), "s"(m0b), "i"(imm), "s"(mask)                                                                                         \
               : "memory", "scc")
#define TOUCH(rs, voff, soff) asm volatile("s_mov_b32 m0, %3\n\ts_nop 0\n\tbuffer_load_dword %0, %1, %2 offen lds" ::"v"(voff), "s"(rs), "s"(soff), "s"(L_MISC1 + 4096) : "memory")
#define LD_SC1(dst, rs, voff, soff, imm) asm volatile("buffer_load_dwordx4 %0, %1, %2, %3 offen offset:%4 sc1" : "=v"(dst) : "v"(voff), "s"(rs), "s"(soff), "i"(imm) : "memory")
#define WAITV(n) asm volatile("s_waitcnt vmcnt(%0)" ::"i"(n) : "memory")
#define WAITV1(n, a) asm volatile("s_waitcnt vmcnt(%1)" : "+v"(a) : "i"(n) : "memory")
#define WAITV2(n, a, b) asm volatile("s_waitcnt vmcnt(%2)" : "+v"(a), "+v"(b) : "i"(n) : "memory")
#define SB() __builtin_amdgcn_sched_barrier(0)
#define LAUNDER(x) asm volatile("" : "+v"(x))
#define BAR() asm volatile("s_waitcnt lgkmcnt(0)\n\ts_barrier" ::: "memory")
DI void st_sc1_128(v4u rs, u32 voff, v4u v) { asm volatile("buffer_store_dwordx4 %0, %1, %2, 0 offen sc1\n\ts_nop 1" ::"v"(v), "v"(voff), "s"(rs) : "memory"); }
#define ST32M(rs, voff, v, mask) asm volatile("s_mov_b64 exec, %3\n\tbuffer_store_dword %0, %1, %2, 0 offen sc1\n\ts_mov_b64 exec, -1" ::"v"(v), "v"(voff), "s"(rs), "i"(mask) : "memory")
#define ST128M(rs, voff, v, mask) asm volatile("s_mov_b64 exec, %3\n\tbuffer_store_dwordx4 %0, %1, %2, 0 offen sc1\n\ts_mov_b64 exec, -1\n\ts_nop 0" ::"v"(v), "v"(voff), "s"(rs), "i"(mask) : "memory")
DI void st_sc1_32(v4u rs, u32 voff, u32 v) { asm volatile("buffer_store_dword %0, %1, %2, 0 offen sc1" ::"v"(v), "v"(voff), "s"(rs) : "memory"); }

DI f4 mfma32(v4u a, v4u b, f4 c) { return __builtin_amdgcn_mfma_f32_16x16x32_bf16(__builtin_bit_cast(v8bf, a), __builtin_bit_cast(v8bf, b), c, 0, 0, 0); }
DI f4 mfma16(v2u a, v2u b, f4 c) { return __builtin_amdgcn_mfma_f32_16x16x16bf16_1k(__builtin_bit_cast(v4s, a), __builtin_bit_cast(v4s, b), c, 0, 0, 0); }
DI u32 pk(float a, float b) {
  f2 v = {a, b};
  return __builtin_bit_cast(u32, __builtin_convertvector(v, v2bf));
}
DI u32 pk2(f2 v) { return __builtin_bit_cast(u32, __builtin_convertvector(v, v2bf)); }
DI f2 lo2(f4 v) { return __builtin_shufflevector(v, v, 0, 1); }
DI f2 hi2(f4 v) { return __builtin_shufflevector(v, v, 2, 3); }
DI f2 fma2(f2 a, f2 b, f2 c) { return __builtin_elementwise_fma(a, b, c); }
DI f2 bc2(float x) { return f2{x, x}; }
DI float blo(u32 v) { return __uint_as_float(v << 16); }
DI float bhi(u32 v) { return __uint_as_float(v & 0xffff0000u); }
DI float dot2(u32 a, u32 b, float c) { return __builtin_amdgcn_fdot2_f32_bf16(__builtin_bit_cast(v2bf, a), __builtin_bit_cast(v2bf, b), c, false); }
DI v4u lds128(u32 a) { return *(LAS v4u*)a; }
DI f4 lds128f(u32 a) { return *(LAS f4*)a; }
DI v2u trd(u32 a) { return __builtin_bit_cast(v2u, __builtin_amdgcn_ds_read_tr16_b64_v4i16((LAS v4s*)a)); }
DI v4u trfrag(u32 a) {
  v2u x = trd(a), y = trd(a + 512);
  v4u r = {x.x, x.y, y.x, y.y};
  return r;
}
template <int CTRL> DI float dppf(float x) { return __uint_as_float(__builtin_amdgcn_mov_dpp(__float_as_uint(x), CTRL, 0xf, 0xf, true)); }
DI float gsum(float x) {
  auto a = __builtin_amdgcn_permlane16_swap(__float_as_uint(x), __float_as_uint(x), false, false);
  x = __uint_as_float(a[0]) + __uint_as_float(a[1]);
  auto b = __builtin_amdgcn_permlane32_swap(__float_as_uint(x), __float_as_uint(x), false, false);
  return __uint_as_float(b[0]) + __uint_as_float(b[1]);
}
DI float fmx(float a, float b) { return __builtin_elementwise_maximum(a, b); }
DI float sw16max(float x) {
  auto a = __builtin_amdgcn_permlane16_swap(__float_as_uint(x), __float_as_uint(x), false, false);
  return fmx(__uint_as_float(a[0]), __uint_as_float(a[1]));
}
DI float sw32max(float x) {
  auto a = __builtin_amdgcn_permlane32_swap(__float_as_uint(x), __float_as_uint(x), false, false);
  return fmx(__uint_as_float(a[0]), __uint_as_float(a[1]));
}
DI float sw16add(float x) {
  auto a = __builtin_amdgcn_permlane16_swap(__float_as_uint(x), __float_as_uint(x), false, false);
  return __uint_as_float(a[0]) + __uint_as_float(a[1]);
}
DI float sw32add(float x) {
  auto a = __builtin_amdgcn_permlane32_swap(__float_as_uint(x), __float_as_uint(x), false, false);
  return __uint_as_float(a[0]) + __uint_as_float(a[1]);
}
DI float wsum(float x) {
  x += dppf<0xB1>(x);
  x += dppf<0x4E>(x);
  x += dppf<0x141>(x);
  x += dppf<0x140>(x);
  return gsum(x);
}
DI void wsum2(float& x, float& y) {
  x += dppf<0xB1>(x); y += dppf<0xB1>(y);
  x += dppf<0x4E>(x); y += dppf<0x4E>(y);
  x += dppf<0x141>(x); y += dppf<0x141>(y);
  x += dppf<0x140>(x); y += dppf<0x140>(y);
  x = sw16add(x); y = sw16add(y);
  x = sw32add(x); y = sw32add(y);
}
DI v4u widen(v2u a, v2u b) {
  auto x = __builtin_amdgcn_permlane16_swap(a.x, b.x, false, false);
  auto y = __builtin_amdgcn_permlane16_swap(a.y, b.y, false, false);
  v4u r = {x[0], y[0], x[1], y[1]};
  return r;
}

#define STAMP(i)

#define TS(k)
#define TSFIN(n)
#define U(k)
struct Args {
  const void *x, *wqkv, *wo, *w1, *b1, *w2, *b2;
  void *out, *ws;
};

__global__ __launch_bounds__(512, 2) void fused_kernel(Args a) {
  STAMP(0);
  const int tid = threadIdx.x, l = tid & 63, w = __builtin_amdgcn_readfirstlane(tid >> 6);
  const int b = blockIdx.x, xcd = b & 7, bi = b >> 3;
  {
    u64 pc;
    asm volatile("s_getpc_b64 %0" : "=s"(pc));
    const v4u rc = mkrs((const void*)(pc & ~127ull), CODE_BYTES);
    asm volatile("s_mov_b32 m0, %2\n\ts_mov_b64 exec, 1\n\tbuffer_load_dword off, %0, %1 lds\n\ts_mov_b64 exec, -1" ::"s"(rc), "s"((bi * 8 + w) * 128), "s"(L_MISC1 + 4096));
  }
  const int c = l & 15, g = l >> 4, q4 = (l & 15) >> 2, p4 = l & 3;
  const int wq_ = w & 3, wh = w >> 2;
  const int R = 8 * (xcd >> 1) + (bi >> 2), C = 4 * (xcd & 1) + (bi & 3);
  const int FT = 7 * (xcd >> 1) + (bi >> 2);
  const bool p3 = __builtin_expect((bi >> 2) < 7, 1);
  u32 avo = l * 16;
  u32 vxa = (l >> 3) * 1024 + (((l & 7) ^ (l >> 3)) << 4);
  u32 vxh = vxa ^ ((wq_ & 1) << 6);
  u32 vwb = (8 * w + (l >> 3)) * 3072 + (((l & 7) ^ ((((l >> 4) & 1) << 1) | ((w & 1) << 2))) << 4);
  const int r0 = R * 64 - 16;
  u32 xrow_a = (u32)max(r0 + 16 * wq_, 0) * 1024u, xrow_b = xrow_a + 8192u, xrow_h = (u32)(r0 + 64 + 4 * wq_) * 1024u;
  const int xr1 = ((bi >> 2) + (XMASK == 7 ? 2 : 1) * (bi & 3)) & XMASK, xo1 = (XOFF * ((xcd >> 1) * 2 + (xcd & 1) * 4)) & 7;
  u32 mx2k = wq_ * 2048, mxh = wq_ * 512, mw3k = w * 3072, mw1k = w * 1024;
  const u64 mask0 = (wq_ == 0) ? ~0xFFull : ~0ull;
  const u64 maskh = 0xFFFFFFFFull;
#define KC(ch) ((((ch) ^ xr1) + xo1) & 7)
#define stage_xa(ch) dma1m(rx, vxa, xrow_a + KC(ch) * 128, mx2k, ((ch)&3) * STG, mask0)
#define stage_xb(ch) dma1(rx, vxa, xrow_b + KC(ch) * 128, mx2k, ((ch)&3) * STG + 1024)
#define stage_xh(ch) dma1m(rx, vxh, xrow_h + KC(ch) * 128, mxh, ((ch)&3) * STG + 8192, maskh)
#define stage_x(ch) { stage_xa(ch); stage_xb(ch); stage_xh(ch); }
#define stage_w(ch) dma3(rqkv, vwb, KC(ch) * (64 * 3072) + C * 128, mw3k, ((ch)&3) * STG + 10240)
#define stage_w1(ch, i) dma1o(rqkv, vwb, KC(ch) * (64 * 3072) + C * 128, mw3k, ((ch)&3) * STG + 10240, (i)*1024)
  const u32 ONES = 0x3F803F80u;
  u32 xrso = (R * 64 + 8 * C + w) * 1024, kc0x = KC(0) * 128, kc0w = KC(0) * (64 * 3072) + C * 128;
  asm volatile("" : "+v"(avo), "+v"(vxa), "+v"(vxh), "+v"(vwb));
  asm volatile("" : "+s"(xrow_a), "+s"(xrow_b), "+s"(xrow_h), "+s"(mx2k), "+s"(mxh), "+s"(mw3k), "+s"(xrso), "+s"(kc0x), "+s"(kc0w));
  SB();
  const v4u rx = mkrs(a.x, S_ * D_ * 2), rqkv = mkrs(a.wqkv, D_ * 1536 * 2), rwo = mkrs(a.wo, D_ * D_ * 2), rw1 = mkrs(a.w1, ((ABL & 16) || !p3) ? 0 : S_ * F_ * 2),
            rw2 = mkrs(a.w2, (ABL & 16) ? 0 : F_ * S_ * 2), rws = mkrs(a.ws, WS_BYTES);
  u32 tag;
  u64 tag2;
  asm volatile("s_load_dword %0, %1, 0x0" : "=s"(tag) : "s"(a.ws));

  if (BADC && w == 3) {
    const u32 cb = (3u - (rqkv.x >> 7)) & 7u;
    if ((cb >> 2) == (u32)(xcd & 1)) TOUCH(rqkv, (l >> 2) * 3072 + min(l & 3, 2) * 1024, bi * (16 * 3072) + cb * 128);
  }
  if (wh == 1) {
    dma1m(rx, vxa, xrow_a + kc0x, mx2k, 0, mask0);
    dma1(rx, vxa, xrow_b + kc0x, mx2k, 1024);
    dma1m(rx, vxh, xrow_h + kc0x, mxh, 8192, maskh);
  }
  v4u xr;
  asm volatile("buffer_load_dwordx4 %0, %1, %2, %3 offen" : "=v"(xr) : "v"(avo), "s"(rx), "s"(xrso) : "memory");
  dma3(rqkv, vwb, kc0w, mw3k, 10240);
  STAMP(21);
  if (tid < 32) {
    *(LAS v4u*)((tid >> 3) * STG + (tid & 7) * 16) = v4u{ONES, ONES, ONES, ONES};
    *(LAS v2u*)(L_CNT) = v2u{0u, 0u};
  }

  u32 vw1a, vw1b, vw2a, vw2b, vwoa, vwob;
#define PRE_W()                                                                                        \
  {                                                                                                    \
    u32 lr = l >> 3;                                                                                   \
    LAUNDER(lr);                                                                                       \
    const u32 vsw = ((l & 7) ^ (lr & 2)) << 4;                                                         \
    vw1a = lr * 3584 + vsw; vw1b = (vw1a ^ 64u) + 8 * 3584 - 1024;                                     \
    vw2a = lr * 4096 + vsw; vw2b = (vw2a ^ 64u) + 8 * 4096 - 1024;                                     \
    vwoa = lr * 1024 + vsw; vwob = (vwoa ^ 64u) + 8 * 1024 - 1024;                                     \
    LAUNDER(vw1a); LAUNDER(vw1b); LAUNDER(vw2a); LAUNDER(vw2b); LAUNDER(vwoa); LAUNDER(vwob);          \
  }
  const u32 fl = ((q4 >> 1) << 1) | ((g & 1) << 2);
  const u32 trl = (fl + (p4 >> 1)) * 16 + 8 * (p4 & 1);
  u32 k3lo, k3hi, k4lo, k4hi, w1q, w2q, w1h, w2h;
#define PRE_K()                                                                                                            \
  {                                                                                                                        \
    u32 bb = b;                                                                                                            \
    asm volatile("" : "+s"(bb));                                                                                           \
    const u32 bi = bb >> 3, ax = (AX) ^ (BX), r3 = R3, r4 = R4;                       \
    const u64 t3 = PERM3 ^ ((ax ^ r3 ^ (BX3)) * 0x1111111111111111ull);                                                            \
    const u64 cx = bi & 3;          \
    const u64 t4 = K4MODE == 0 ? ((0x00DCBA9876543210ull >> (4 * r4)) | (0x00DCBA9876543210ull << (56 - 4 * r4)))          \
                               : ((10 + cx) | ((0x9876543210ull ^ ((K4X) * 0x1111111111ull)) << 4) | ((10 + (cx ^ 1)) << 44) | ((10 + (cx ^ 2)) << 48) | ((10 + (cx ^ 3)) << 52)); \
    k3lo = (u32)t3; k3hi = (u32)(t3 >> 32); k4lo = (u32)t4; k4hi = (u32)(t4 >> 32);                                        \
    w1q = (16 * wq_ * 32) * 3584 + FT * 128; w2q = (14 * wq_ * 32) * 4096 + R * 128;                                       \
    w1h = w1q + 16 * wh * 3584; w2h = w2q + 16 * wh * 4096;                                                                \
    asm volatile("" : "+s"(k3lo), "+s"(k3hi), "+s"(k4lo), "+s"(k4hi), "+s"(w1q), "+s"(w2q), "+s"(w1h), "+s"(w2h));         \
  }
#define KS3(j) ((((j) < 8 ? k3lo : k3hi) >> (4 * ((j)&7))) & 15u)
#define KS4(j) ((((j) < 8 ? k4lo : k4hi) >> (4 * ((j)&7))) & 15u)
  const u32 mslot = wq_ * 4096 + wh * 2048, mw4k = w * 4096, mq4k = wq_ * 4096;
#define w1_issue(j, slot) dma2p(j, rw1, vw1a, vw1b, w1h + KS3(j) * (32 * 3584), mslot, (slot)*16384)
#define w2_issue(j, slot) dma2(rw2, vw2a, vw2b, w2h + KS4(j) * (32 * 4096), mslot, (slot)*16384)
#define w1_issue4(j, slot) { const u32 so_ = w1q + KS3(j) * (32 * 3584); dma2p(j, rw1, vw1a, vw1b, so_, mq4k, (slot)*16384); dma2p(j, rw1, vw1a, vw1b, so_ + 16 * 3584, mq4k, (slot)*16384 + 2048); }
#define w2_issue4(j, slot) { const u32 so_ = w2q + KS4(j) * (32 * 4096); dma2(rw2, vw2a, vw2b, so_, mq4k, (slot)*16384); dma2(rw2, vw2a, vw2b, so_ + 16 * 4096, mq4k, (slot)*16384 + 2048); }
#define SWEEP1(idx, par) asm volatile("s_mov_b32 m0, %3\n\ts_nop 0\n\tbuffer_load_dword %0, %1, %2 offen lds" ::"v"((l >> 3) * 4096 + (l & 7) * 128), "s"(rw2), \
    "s"((448 * (bi & 3) + 16 * (idx) + 8 * (par)) * 4096 + (xcd >> 1) * 1024), "s"(L_M + 3072 + wq_ * 4096) : "memory")
#define wo_issue(half, part) dma2(rwo, vwoa, vwob, ((8 * (half) + w) * 32 + 16 * (part)) * 1024 + C * 128, mw4k, (half)*32768 + (part)*2048)
#define PSTAMP(k)
#define SPOLL8(off_)                                                                                                                    \
  {                                                                                                                                     \
    u32 n_;                                                                                                                             \
    asm volatile(                                                                                                                       \
        "s_mov_b32 %0, 0\n1:\n\ts_buffer_load_dwordx8 s[84:91], %1, %2 glc\n\ts_add_u32 %0, %0, 1\n\ts_waitcnt lgkmcnt(0)\n\t"         \
        "s_xor_b64 s[84:85], s[84:85], %3\n\ts_xor_b64 s[86:87], s[86:87], %3\n\ts_xor_b64 s[88:89], s[88:89], %3\n\t"                  \
        "s_xor_b64 s[90:91], s[90:91], %3\n\ts_or_b64 s[84:85], s[84:85], s[86:87]\n\ts_or_b64 s[88:89], s[88:89], s[90:91]\n\t"        \
        "s_or_b64 s[84:85], s[84:85], s[88:89]\n\ts_cselect_b32 s84, %0, 0x10000\n\ts_cmp_lt_u32 s84, 0x10000\n\ts_cbranch_scc1 1b"     \
        : "=&s"(n_) : "s"(rws), "s"(off_), "s"(tag2) : "memory", "scc", "s84", "s85", "s86", "s87", "s88", "s89", "s90", "s91");        \
  }
#define SPOLL4(off_)                                                                                                                    \
  {                                                                                                                                     \
    u32 n_;                                                                                                                             \
    asm volatile(                                                                                                                       \
        "s_mov_b32 %0, 0\n1:\n\ts_buffer_load_dwordx4 s[84:87], %1, %2 glc\n\ts_add_u32 %0, %0, 1\n\ts_waitcnt lgkmcnt(0)\n\t"         \
        "s_xor_b64 s[84:85], s[84:85], %3\n\ts_xor_b64 s[86:87], s[86:87], %3\n\t"                                                      \
        "s_or_b64 s[84:85], s[84:85], s[86:87]\n\ts_cselect_b32 s84, %0, 0x10000\n\ts_cmp_lt_u32 s84, 0x10000\n\ts_cbranch_scc1 1b"     \
        : "=&s"(n_) : "s"(rws), "s"(off_), "s"(tag2) : "memory", "scc", "s84", "s85", "s86", "s87");                                    \
  }
#define XWAIT()                                                                                              \
  _Pragma("nounroll") for (;;) {                                                                             \
    u32 cnt;                                                                                                 \
    asm volatile("ds_read_b32 %0, %1 offset:4\n\ts_waitcnt lgkmcnt(0)" : "=v"(cnt) : "v"(cna) : "memory"); \
    if (__builtin_amdgcn_readfirstlane(cnt) == 4) break;                                                     \
    __builtin_amdgcn_s_sleep(1);                                                                             \
  }
#define POLL(vo_, k)                                                                                                                    \
  _Pragma("nounroll") for (u32 sp = 0; sp < SPIN_CAP; ++sp) {                                                                           \
    u32 f;                                                                                                                              \
    asm volatile("buffer_load_dword %0, %1, %2, 0 offen sc1\n\ts_waitcnt vmcnt(0)" : "=v"(f) : "v"(vo_), "s"(rws) : "memory");         \
    PSTAMP(k)                                                                                                                           \
    if (__all(f == tag)) break;                                                                                                         \
  }
  u32 st_o, st_h, st_u;
#define PRE_ST()                                                                                        \
  {                                                                                                     \
    u32 ll = l;                                                                                         \
    LAUNDER(ll);                                                                                        \
    const u32 fso = ((ll & 15) + 16 * ((wq_ & 1) * 2 + (ll >> 5))) * 16, gp = (ll >> 4) & 1;            \
    st_o = WS_OF + ((4 * R + 2 * wh + gp) * 16 + 2 * C + (wq_ >> 1)) * 1024 + fso;                      \
    st_h = WS_HF + ((4 * C + 2 * wh + gp) * 64 + 2 * R + (wq_ >> 1)) * 1024 + fso;                      \
    st_u = WS_UF + ((4 * C + 2 * wh + gp) * 56 + 2 * FT + (wq_ >> 1)) * 1024 + fso;                     \
    LAUNDER(st_o); LAUNDER(st_h); LAUNDER(st_u);                                                        \
  }

  constexpr u32 L_XRES = 4 * STG;
  v2u b1v;
  u32 b2v;
  PRE_W() PRE_K() PRE_ST()
  const u32 own = L_M + wq_ * 4096;
  u32 rda = own + l * 16;
  u32 gso = WS_SX + (R * 4 + wq_) * 2048 + C * 256 + c * 16, fco = WS_FLAGC + ((8 * wq_ + (l & 7)) * 8 + C) * 128 + R * 4;
  v2u idm = {q4 != g ? 0u : (p4 == 0 ? 0x3F80u : (p4 == 1 ? 0x3F800000u : 0u)), q4 != g ? 0u : (p4 == 2 ? 0x3F80u : (p4 == 3 ? 0x3F800000u : 0u))};
  LAUNDER(idm);
  u32 st_h2 = st_h + 131072;
  gso += (g & 1) * 8;
  LAUNDER(st_h2); LAUNDER(rda); LAUNDER(gso); LAUNDER(fco);
  u32 so = WS_OF + ((4 * R + wq_) * 16 + 8 * wh) * 1024, so2 = so + 4096, sxb = WS_SX + (R * 4 + wq_) * 2048;
  asm volatile("" : "+s"(so), "+s"(so2), "+s"(sxb));
  u32 wt0 = wh * 32768 + (8 * g + q4) * 128 + trl, xra_ = L_XRES + (16 * wq_ + c) * 128 + (((g >> 1) ^ (c & 7)) << 4) + 8 * (g & 1);
  u32 fao = WS_FLAGA + (R * 8 + (l & 7)) * 128 + C * 4, cna = L_CNT;
  LAUNDER(wt0); LAUNDER(xra_); LAUNDER(fao); LAUNDER(cna);
  {
    u32 vgs = WS_SX1 + (R * 64 + 8 * C + w) * 16;
    LAUNDER(vgs);
    asm volatile("s_waitcnt lgkmcnt(0)" : "+s"(tag)::"memory");
    tag += 0x9E3779B9u;
    tag2 = (u64)tag * 0x100000001ull;
    WAITV1(3, xr);
    const f2 pv = bc2(__uint_as_float(__builtin_amdgcn_readfirstlane(xr.x << 16)));
    const f2 d0 = f2{blo(xr.x), bhi(xr.x)} - pv, d1 = f2{blo(xr.y), bhi(xr.y)} - pv, d2 = f2{blo(xr.z), bhi(xr.z)} - pv, d3 = f2{blo(xr.w), bhi(xr.w)} - pv;
    const f2 sv = (d0 + d1) + (d2 + d3), qv = fma2(d0, d0, d1 * d1) + fma2(d2, d2, d3 * d3);
    float s1 = sv.x + sv.y, s2 = qv.x + qv.y;
    wsum2(s1, s2);
    const float md = s1 * (1.f / 512.f), var = fmx(s2 * (1.f / 512.f) - md * md, 0.f);
    const v4u gr = {__float_as_uint(__builtin_amdgcn_rsqf(var + 1e-5f)), tag, __float_as_uint(pv.x + md), tag};
    ST128M(rws, vgs, gr, 1);
    stage_w(1);
    const v4u rb1 = mkrs(a.b1, F_ * 2), rb2 = mkrs(a.b2, S_ * 2);
    asm volatile("buffer_load_dwordx2 %0, %1, %2, %3 offen" : "=v"(b1v) : "v"(g * 8), "s"(rb1), "s"((FT * 64 + 16 * wq_) * 2) : "memory");
    asm volatile("buffer_load_ushort %0, %1, %2, %3 offen" : "=v"(b2v) : "v"(c * 2), "s"(rb2), "s"((R * 64 + 16 * wq_) * 2) : "memory");
  }
#define PBAR(n) { WAITV(n); BAR(); }
  if (wh == 0) {
    const bool odd = c & 1;
    const bool cur0 = (4 * g + (c & 1)) <= c, cur1 = (4 * g + 2 + (c & 1)) <= c;
    u32 xa[2][2], wa[2];
    xa[0][0] = c * 128 + ((g ^ (c & 7)) << 4);
    xa[0][1] = xa[0][0] ^ 64;
    wa[0] = 10240 + g * 3072 + q4 * 128 + (trl ^ (wq_ << 5));
    xa[1][0] = xa[0][0] + 2 * STG; xa[1][1] = xa[0][1] + 2 * STG; wa[1] = wa[0] + 2 * STG;
    LAUNDER(xa[0][0]); LAUNDER(xa[0][1]); LAUNDER(wa[0]);
    LAUNDER(xa[1][0]); LAUNDER(xa[1][1]); LAUNDER(wa[1]);
    u32 tb = L_MISC1 + c * 4, tv = L_MISC1 + g * 16, tk0 = tv + (cur0 ? 64 : 0) + (c & 1) * 4, tk1 = tv + (cur1 ? 72 : 8) + (c & 1) * 4;
    LAUNDER(tb); LAUNDER(tk0); LAUNDER(tk1); LAUNDER(tv);
    const u32 z_ = 0x0c0c0c0cu, s0_ = odd ? 0x01000c0cu : 0x0c0c0100u, s1_ = odd ? 0x03020c0cu : 0x0c0c0302u;
    u32 sel0 = cur0 ? z_ : s0_, sel1 = cur1 ? z_ : s1_, sel2 = cur0 ? s0_ : z_, sel3 = cur1 ? s1_ : z_;
    float na0 = (R == 0 && !cur0) ? -3.0e38f : 0.f, na1 = (R == 0 && !cur1) ? -3.0e38f : 0.f;
    LAUNDER(sel0); LAUNDER(sel1); LAUNDER(sel2); LAUNDER(sel3); LAUNDER(na0); LAUNDER(na1);
    f4 aQ[5], aK[5], aV[5];
#pragma unroll
    for (int i = 0; i < 5; ++i) aQ[i] = aK[i] = aV[i] = f4{0, 0, 0, 0};
    float ml[5], rq[4];
    f2 rk[4], fs[4];
    f4 mv[5];
    v4u fx[2][5], fw[2][3];
#define HI(s) ((((s) >> 1) >> 1) & 1)
#define BS(s) ((((s) >> 1) & 1) * STG)
#define RW(s, i) fw[(s)&1][i] = trfrag(wa[HI(s)] + BS(s) + ((s)&1) * 12288 + (i)*1024)
#define RX(s, i) fx[(s)&1][i] = lds128(xa[HI(s)][(s)&1] + BS(s) + (i)*2048)
#define MQ(s, i) { aQ[i] = mfma32(fw[(s)&1][0], fx[(s)&1][i], aQ[i]); LAUNDER(aQ[i]); }
#define MK(s, i) { aK[i] = mfma32(fw[(s)&1][1], fx[(s)&1][i], aK[i]); LAUNDER(aK[i]); }
#define MV(s, i) { aV[i] = mfma32(fx[(s)&1][i], fw[(s)&1][2], aV[i]); LAUNDER(aV[i]); }
#define P1S(s, BARX, D1, D2)                                                      \
  {                                                                               \
    TS(s)                                                                         \
    MQ(s, 0); MQ(s, 1); MQ(s, 2); SB();                                           \
    BARX; SB();                                                                   \
    RW((s) + 1, 0); SB(); MQ(s, 3); SB();                                         \
    RX((s) + 1, 0); SB(); MQ(s, 4); SB();                                         \
    RX((s) + 1, 1); SB(); MK(s, 0); SB();                                         \
    RX((s) + 1, 2); SB(); MK(s, 1); SB();                                         \
    RX((s) + 1, 3); SB(); MK(s, 2); SB();                                         \
    RX((s) + 1, 4); SB(); MK(s, 3); SB();                                         \
    RW((s) + 1, 1); SB(); MK(s, 4); MV(s, 0); SB();                               \
    RW((s) + 1, 2); SB(); MV(s, 1); SB();                                         \
    D1; SB(); MV(s, 2); MV(s, 3); SB();                                           \
    D2; SB(); MV(s, 4); SB();                                                     \
  }
#define TAB1() { _Pragma("unroll") for (int i = 0; i < 4; ++i) rq[i] = *(LAS float*)(tb + 64 + 64 * i); }
#define TAB2() { _Pragma("unroll") for (int i = 0; i < 4; ++i) rk[i].x = *(LAS float*)(tk0 + 64 * i); }
#define TAB3() { _Pragma("unroll") for (int i = 0; i < 4; ++i) rk[i].y = *(LAS float*)(tk1 + 64 * i); }
#define TAB4() { _Pragma("unroll") for (int i = 0; i < 5; ++i) ml[i] = *(LAS float*)(tb + 512 + 64 * i); }
#define TAB5() { _Pragma("unroll") for (int i = 0; i < 5; ++i) mv[i] = lds128f(tv + 512 + 64 * i); }
#define TAB6() { _Pragma("unroll") for (int i = 0; i < 4; ++i) { fs[i] = rk[i] * (rq[i] * (0.25f * 1.44269504088896f)); LAUNDER(fs[i]); } }
    STAMP(22);
    PBAR(1 + 3 + 2)
    STAMP(1);
    RW(0, 0); RX(0, 0); RX(0, 1); RX(0, 2); RX(0, 3); RX(0, 4); RW(0, 1); RW(0, 2); SB();
    P1S(0, , , )
    P1S(1, PBAR(0), , ) P1S(2, , , )
    P1S(3, BAR(), , ) P1S(4, , , )
    P1S(5, BAR(), , ) P1S(6, , , )
    STAMP(16);
    P1S(7, BAR(), , ) P1S(8, , , )
    P1S(9, BAR(), , ) P1S(10, , , )
    P1S(11, BAR(), TAB1(), TAB2()) P1S(12, , TAB3(), TAB4())
    P1S(13, BAR(), TAB6(), ) P1S(14, , TAB5(), )
    TS(15)
    MQ(15, 0); MK(15, 0); MV(15, 0); MQ(15, 1); MK(15, 1); MQ(15, 2); MK(15, 2); SB();
    f4 csq, csk;
#pragma unroll
    for (int i = 0; i < 4; ++i) csq[i] = dppf<0x150>(aQ[0][i]);
    SB();
    MQ(15, 3); MK(15, 3); SB();
#pragma unroll
    for (int i = 0; i < 4; ++i) csk[i] = dppf<0x150>(aK[0][i]);
    SB();
    MQ(15, 4); MK(15, 4); SB();
    float csv;
    {
      auto t1 = __builtin_amdgcn_permlane16_swap(__float_as_uint(aV[0][0]), __float_as_uint(aV[0][0]), false, false);
      auto t2 = __builtin_amdgcn_permlane32_swap(t1[0], t1[0], false, false);
      csv = -__uint_as_float(t2[0]);
    }
    auto foldT = [&](f4 acc, float m, f4 cs) {
      v2u r;
      r.x = pk2(fma2(bc2(-m), lo2(cs), lo2(acc)));
      r.y = pk2(fma2(bc2(-m), hi2(cs), hi2(acc)));
      return r;
    };
    auto foldV = [&](f4 acc, f4 m) {
      v2u r;
      r.x = pk2(fma2(lo2(m), bc2(csv), lo2(acc)));
      r.y = pk2(fma2(hi2(m), bc2(csv), hi2(acc)));
      return r;
    };
    const f4 z4 = {0, 0, 0, 0};
    v2u Q[5], K[5], V[5];
    f4 sp[4], sc[4];
    MV(15, 1); SB();
    Q[1] = foldT(aQ[1], ml[1], csq); K[0] = foldT(aK[0], ml[0], csk); SB();
    MV(15, 2); SB();
    K[1] = foldT(aK[1], ml[1], csk); Q[2] = foldT(aQ[2], ml[2], csq); SB();
    MV(15, 3); sp[0] = mfma16(K[0], Q[1], z4); SB();
    K[2] = foldT(aK[2], ml[2], csk); Q[3] = foldT(aQ[3], ml[3], csq); SB();
    MV(15, 4); sc[0] = mfma16(K[1], Q[1], z4); sp[1] = mfma16(K[1], Q[2], z4); SB();
    TS(16)
    STAMP(2);
    K[3] = foldT(aK[3], ml[3], csk); Q[4] = foldT(aQ[4], ml[4], csq); SB();
    sc[1] = mfma16(K[2], Q[2], z4); sp[2] = mfma16(K[2], Q[3], z4); SB();
    K[4] = foldT(aK[4], ml[4], csk); V[0] = foldV(aV[0], mv[0]); SB();
    sc[2] = mfma16(K[3], Q[3], z4); sp[3] = mfma16(K[3], Q[4], z4); SB();
    V[1] = foldV(aV[1], mv[1]); SB();
    sc[3] = mfma16(K[4], Q[4], z4); SB();
    V[2] = foldV(aV[2], mv[2]); V[3] = foldV(aV[3], mv[3]); V[4] = foldV(aV[4], mv[4]);
    SB();
    f2 t[4];
#pragma unroll
    for (int i = 0; i < 4; ++i) {
      const f4 cc_ = sc[i], pp_ = sp[i];
      const float c0 = cc_[0], c1 = cc_[1], c2 = cc_[2], c3 = cc_[3], p0 = pp_[0], p1 = pp_[1], p2 = pp_[2], p3_ = pp_[3];
      const float ca = odd ? c1 : c0, cb = odd ? c3 : c2, pa = odd ? p1 : p0, pb = odd ? p3_ : p2;
      t[i].x = cur0 ? ca : pa;
      t[i].y = cur1 ? cb : pb;
      t[i] = (i == 0) ? fma2(t[i], fs[i], f2{na0, na1}) : t[i] * fs[i];
    }
    SB();
    auto m1a = __builtin_amdgcn_permlane16_swap(__float_as_uint(fmx(t[0].x, t[0].y)), __float_as_uint(fmx(t[1].x, t[1].y)), false, false);
    auto m1b = __builtin_amdgcn_permlane16_swap(__float_as_uint(fmx(t[2].x, t[2].y)), __float_as_uint(fmx(t[3].x, t[3].y)), false, false);
    auto m2 = __builtin_amdgcn_permlane32_swap(__float_as_uint(fmx(__uint_as_float(m1a[0]), __uint_as_float(m1a[1]))),
                                               __float_as_uint(fmx(__uint_as_float(m1b[0]), __uint_as_float(m1b[1]))), false, false);
    const u32 m3 = __float_as_uint(fmx(__uint_as_float(m2[0]), __uint_as_float(m2[1])));
    auto m4 = __builtin_amdgcn_permlane32_swap(m3, m3, false, false);
    auto m5a = __builtin_amdgcn_permlane16_swap(m4[0], m4[0], false, false);
    auto m5b = __builtin_amdgcn_permlane16_swap(m4[1], m4[1], false, false);
    const float mxv[4] = {__uint_as_float(m5a[0]), __uint_as_float(m5a[1]), __uint_as_float(m5b[0]), __uint_as_float(m5b[1])};
    f2 e[4];
    f4 o4[4];
#pragma unroll
    for (int i = 0; i < 4; ++i) {
      const f2 d = t[i] - bc2(mxv[i]);
      e[i] = f2{__builtin_amdgcn_exp2f(d.x), __builtin_amdgcn_exp2f(d.y)};
    }
    SB();
#pragma unroll
    for (int i = 0; i < 4; ++i) {
      const u32 pp = pk2(e[i] * rk[i]);
      const v4u P = {__builtin_amdgcn_perm(0, pp, sel0), __builtin_amdgcn_perm(0, pp, sel1), __builtin_amdgcn_perm(0, pp, sel2), __builtin_amdgcn_perm(0, pp, sel3)};
      const v4u Vv = {V[i].x, V[i].y, V[i + 1].x, V[i + 1].y};
      o4[i] = mfma32(Vv, P, z4);
    }
    SB();
    auto s1a = __builtin_amdgcn_permlane16_swap(__float_as_uint(e[0].x + e[0].y), __float_as_uint(e[1].x + e[1].y), false, false);
    auto s1b = __builtin_amdgcn_permlane16_swap(__float_as_uint(e[2].x + e[2].y), __float_as_uint(e[3].x + e[3].y), false, false);
    auto s2 = __builtin_amdgcn_permlane32_swap(__float_as_uint(__uint_as_float(s1a[0]) + __uint_as_float(s1a[1])),
                                               __float_as_uint(__uint_as_float(s1b[0]) + __uint_as_float(s1b[1])), false, false);
    const u32 s3 = __float_as_uint(__builtin_amdgcn_rcpf(__uint_as_float(s2[0]) + __uint_as_float(s2[1])));
    auto s4 = __builtin_amdgcn_permlane32_swap(s3, s3, false, false);
    SB();
#pragma unroll
    for (int p = 0; p < 2; ++p) {
      f4 x4, y4;
#pragma unroll
      for (int i = 0; i < 4; ++i) {
        auto tt = __builtin_amdgcn_permlane16_swap(__float_as_uint(o4[2 * p][i]), __float_as_uint(o4[2 * p + 1][i]), false, false);
        x4[i] = __uint_as_float(tt[0]);
        y4[i] = __uint_as_float(tt[1]);
      }
      const f2 inv = bc2(__uint_as_float(s4[p]));
      const v4u ov = {pk2(lo2(x4) * inv), pk2(hi2(x4) * inv), pk2(lo2(y4) * inv), pk2(hi2(y4) * inv)};
      st_sc1_128(rws, st_o + p * 32768, ov);
    }
    TS(17)
    STAMP(3);
  } else {
    v4u gst;
    const int lq = l < 20 ? l : 0;
    u32 gvo = WS_SX1 + (u32)max(r0 + wq_ * 20 + lq, 0) * 16, gta = L_MISC1 + wq_ * 80 + lq * 4;
    u32 vwb2 = vwb - 32 * 3072, vto = ((l >> 2) & 3) * 1024 + (l & 3) * 128;
    const u32 mw3k2 = mw3k - 4 * 3072;
#define stage_v(ch) dma3(rqkv, vwb2, KC(ch) * (64 * 3072) + C * 128, mw3k2, ((ch)&3) * STG + 10240)
#define stage(ch) { stage_x(ch); stage_v(ch); stage_w(ch); }
#define wo_issue2(half, part) dma2(rwo, vwoa, vwob, ((8 * (half) + wq_) * 32 + 16 * (part)) * 1024 + C * 128, mq4k, (half)*32768 + (part)*2048)
    stage_x(1);
    STAMP(22);
    PBAR(1 + 3 + 2 + 3)
    STAMP(1);
    stage(2);
    TOUCH(rwo, vto, (16 * bi + 4 * wq_) * 1024 + (xcd & 1) * 512);
    PBAR(10) stage(3);
    PBAR(10) stage(4);
    LD_SC1(gst, rws, gvo, 0, 0);
    PBAR(10) stage(5);
    STAMP(16);
    PBAR(9) stage(6);
    LAUNDER(gst);
    if (__builtin_expect(!__all(gst.y == tag && gst.w == tag), 0)) {
      _Pragma("nounroll") for (u32 sp = 0; sp < SPIN_CAP; ++sp) {
        asm volatile("buffer_load_dwordx4 %0, %1, %2, 0 offen sc1\n\ts_waitcnt vmcnt(0)" : "=v"(gst) : "v"(gvo), "s"(rws) : "memory");
        if (__all(gst.y == tag && gst.w == tag)) break;
      }
    }
    *(LAS u32*)(gta) = gst.x;
    *(LAS u32*)(gta + 512) = gst.z;
    PBAR(9) stage(7);
    PBAR(9) wo_issue2(0, 0); wo_issue2(0, 1); wo_issue(0, 0); wo_issue(0, 1);
    PBAR(8) wo_issue2(1, 0); wo_issue2(1, 1); wo_issue(1, 0); wo_issue(1, 1);
    dma1(rx, vxa, (R * 64 + 8 * w) * 1024 + C * 128, mw1k, L_XRES);
    dma1(rx, vxa, (R * 64 + 8 * wq_) * 1024 + C * 128, mw1k, L_XRES - 4096);
    STAMP(2);
    w1_issue4(0, 4);
    WAITV(4);
    asm volatile("s_mov_b64 exec, 1\n\tds_add_u32 %0, %1\n\ts_mov_b64 exec, -1" ::"v"(L_CNT), "v"(1u) : "memory");
    STAMP(3);
  }
  TSFIN(18)
  asm volatile("" : "+v"(b1v), "+v"(b2v));
  _Pragma("nounroll") for (;;) {
    u32 cnt;
    asm volatile("ds_read_b32 %0, %1\n\ts_waitcnt lgkmcnt(0)" : "=v"(cnt) : "v"(cna) : "memory");
    if (__builtin_expect(__builtin_amdgcn_readfirstlane(cnt) == 4, 1)) break;
  }
  f4 h0, h1;
  v4u bw[6][4];
  const u32 trb = wq_ * 4096 + (8 * g + q4) * 128 + (trl ^ (wq_ << 5));
#define TRSET(grp) { u32 tg_ = trb + (grp)*65536; LAUNDER(tg_); tr[0] = tg_; tr[1] = tg_ ^ 32; tr[2] = tg_ ^ 64; tr[3] = tg_ ^ 96; }
#define BREAD(dst, slot) _Pragma("unroll") for (int t = 0; t < 4; ++t) dst[t] = trfrag(tr[t] + ((slot)&3) * 16384);
  u32 tr[4];
  {
    v4u wf[8][4];
    {
      const u32 t0 = wt0;
#pragma unroll
      for (int kk = 0; kk < 8; ++kk)
#pragma unroll
        for (int t = 0; t < 4; ++t) wf[kk][t] = trfrag((t0 ^ (t << 5)) + kk * 4096);
    }
    v2u xv[4];
#pragma unroll
    for (int t = 0; t < 4; ++t) xv[t] = *(LAS v2u*)(xra_ ^ (t << 5));
    u32 tagv = tag;
    LAUNDER(tagv);
    if (wh == 0) {
      u32 one = 1u, old;
      LAUNDER(one);
      WAITV(0);
      asm volatile("s_mov_b64 exec, 1\n\tds_add_rtn_u32 %0, %1, %2 offset:4\n\ts_mov_b64 exec, -1\n\ts_waitcnt lgkmcnt(0)" : "=v"(old) : "v"(cna), "v"(one) : "memory");
      if (__builtin_amdgcn_readfirstlane(old) == 3) ST32M(rws, fao, tagv, 255);
      STAMP(4);
    }
    f4 acc[4];
    {
      const u32 km = wh ? 0u : ~0u;
#pragma unroll
      for (int t = 0; t < 4; ++t) {
        acc[t] = f4{blo(xv[t].x & km), bhi(xv[t].x & km), blo(xv[t].y & km), bhi(xv[t].y & km)};
        LAUNDER(acc[t]);
      }
    }
    STAMP(24);
    if (wh == 1) {
      XWAIT()
      STAMP(4);
      w1_issue4(1, 5); w1_issue4(2, 6); w1_issue4(3, 7);
    }
    if (w == 0) SPOLL8(WS_FLAGA + (R * 8 + C) * 128)
    if (NOB & 1) SPOLL4(WS_FLAGA + (R * 8 + C) * 128 + wh * 16) else BAR();
    STAMP(5);
    v4u of[8];
    LD_SC1(of[0], rws, avo, so, 0); LD_SC1(of[1], rws, avo, so, 1024); LD_SC1(of[2], rws, avo, so, 2048); LD_SC1(of[3], rws, avo, so, 3072);
    LD_SC1(of[4], rws, avo, so2, 0); LD_SC1(of[5], rws, avo, so2, 1024); LD_SC1(of[6], rws, avo, so2, 2048); LD_SC1(of[7], rws, avo, so2, 3072);
    SB();
#define SWEEPB() asm volatile("s_mov_b32 m0, %3\n\ts_nop 0\n\tbuffer_load_dword %0, %1, %2 offen lds" ::"v"((l >> 3) * 4096 + (l & 7) * 128), "s"(rw2), \
    "s"((448 * (bi & 3) + 16 * (4 + wq_) + 8 * (xcd & 1)) * 4096 + (xcd >> 1) * 1024), "s"(3 * 16384) : "memory")
#define P2STEP(kk, NY) WAITV1(7 - kk + (NY), of[kk]); _Pragma("unroll") for (int t = 0; t < 4; ++t) acc[t] = mfma32(wf[kk][t], of[kk], acc[t]); SB();
    P2STEP(0, 0) P2STEP(1, 0) P2STEP(2, 0) P2STEP(3, 0) w1_issue(5, 0); SB(); P2STEP(4, 2) P2STEP(5, 2) P2STEP(6, 2) P2STEP(7, 2)
    STAMP(6);
    if (wh == 1) {
#pragma unroll
      for (int t = 0; t < 4; ++t) *(LAS f4*)(rda + t * 1024) = acc[t];
      BAR();
      STAMP(7);
      if (NA2 <= 4) w1_issue4(4, 8);
      if (NA2 > 4) w1_issue4(6, 1);
      TRSET(1) BREAD(bw[0], 4) BREAD(bw[1], 5) BREAD(bw[2], 6) BREAD(bw[3], 7)
      WAITV(NA2 > 4 ? 4 : 0);
      BAR();
      if (NA2 <= 4) w1_issue4(6, 1);
      w1_issue4(7, 2);
      if (NA2 > 4) w1_issue4(8, 3);
      h0 = lds128f(rda + 2048);
      h1 = lds128f(rda + 3072);
      TRSET(2) BREAD(bw[4], 8)
      TRSET(0) BREAD(bw[5], 0)
      STAMP(8);
      BAR();
      if (NA2 <= 4) w1_issue4(8, 3);
      w1_issue4(9, 4);
      if (NA2 > 4) w1_issue4(10, 5);
      BAR();
    } else {
      LAUNDER(acc[0]); LAUNDER(acc[1]); LAUNDER(acc[2]); LAUNDER(acc[3]);
      BAR();
      f4 hh[4];
#pragma unroll
      for (int t = 0; t < 4; ++t) hh[t] = lds128f(rda + t * 1024);
      SB();
#pragma unroll
      for (int t = 0; t < 4; ++t) hh[t] += acc[t];
      const f2 sv = ((lo2(hh[0]) + hi2(hh[0])) + (lo2(hh[1]) + hi2(hh[1]))) + ((lo2(hh[2]) + hi2(hh[2])) + (lo2(hh[3]) + hi2(hh[3])));
      const f2 qv = (fma2(lo2(hh[0]), lo2(hh[0]), hi2(hh[0]) * hi2(hh[0])) + fma2(lo2(hh[1]), lo2(hh[1]), hi2(hh[1]) * hi2(hh[1]))) +
                    (fma2(lo2(hh[2]), lo2(hh[2]), hi2(hh[2]) * hi2(hh[2])) + fma2(lo2(hh[3]), lo2(hh[3]), hi2(hh[3]) * hi2(hh[3])));
      {
        auto t1 = __builtin_amdgcn_permlane16_swap(__float_as_uint(sv.x + sv.y), __float_as_uint(qv.x + qv.y), false, false);
        const v2u gr = {__float_as_uint(sw32add(__uint_as_float(t1[0]) + __uint_as_float(t1[1]))), tagv};
        asm volatile("s_mov_b64 exec, %3\n\tbuffer_store_dwordx2 %0, %1, %2, 0 offen sc1\n\ts_mov_b64 exec, -1" ::"v"(gr), "v"(gso), "s"(rws), "s"(maskh) : "memory");
      }
      STAMP(7);
      h0 = hh[0];
      h1 = hh[1];
      *(LAS f4*)(rda + 2048) = hh[2];
      *(LAS f4*)(rda + 3072) = hh[3];
      TRSET(1) BREAD(bw[0], 4) BREAD(bw[1], 5) BREAD(bw[2], 6) BREAD(bw[3], 7)
      WAITV(PDLY);
      STAMP(28);
      v4u g0, g1;
      LD_SC1(g0, rws, avo, sxb, 0);
      LD_SC1(g1, rws, avo, sxb, 1024);
      BAR();
      TRSET(2) BREAD(bw[4], 8)
      TRSET(0) BREAD(bw[5], 0)
      SB();
      WAITV2(0, g0, g1);
      STAMP(29);
      if (__builtin_expect(!__all(((g0.y ^ tag) | (g0.w ^ tag) | (g1.y ^ tag) | (g1.w ^ tag)) == 0), 0)) {
        _Pragma("nounroll") for (u32 sp = 0; sp < SPIN_CAP; ++sp) {
          asm volatile("buffer_load_dwordx4 %0, %2, %3, %4 offen sc1\n\tbuffer_load_dwordx4 %1, %2, %3, %4 offen offset:1024 sc1\n\ts_waitcnt vmcnt(0)"
                       : "=&v"(g0), "=&v"(g1) : "v"(avo), "s"(rws), "s"(sxb) : "memory");
          if (__all(((g0.y ^ tag) | (g0.w ^ tag) | (g1.y ^ tag) | (g1.w ^ tag)) == 0)) break;
        }
      }
      STAMP(8);
      float m, q2;
      {
        auto t1 = __builtin_amdgcn_permlane16_swap(__float_as_uint(__uint_as_float(g0.x) + __uint_as_float(g1.x)), __float_as_uint(__uint_as_float(g0.z) + __uint_as_float(g1.z)), false, false);
        const u32 t2 = __float_as_uint(sw32add(__uint_as_float(t1[0]) + __uint_as_float(t1[1])) * (1.f / 512.f));
        auto t3 = __builtin_amdgcn_permlane16_swap(t2, t2, false, false);
        m = __uint_as_float(t3[0]);
        q2 = __uint_as_float(t3[1]);
      }
      const float var = fmx(q2 - m * m, 0.f);
      const float r = __builtin_amdgcn_rsqf(fmaf(var + 1e-5f, 1e-5f, var));
      const f2 rr = bc2(r), cc = bc2(-m * r);
      v4u hv[2];
      {
        f4 tt[4];
#pragma unroll
        for (int t = 0; t < 4; ++t) {
          const v2u nn = {pk2(fma2(lo2(hh[t]), rr, cc)), pk2(fma2(hi2(hh[t]), rr, cc))};
          tt[t] = mfma16(nn, idm, f4{0, 0, 0, 0});
        }
#pragma unroll
        for (int p = 0; p < 2; ++p) hv[p] = widen(v2u{pk2(lo2(tt[2 * p])), pk2(hi2(tt[2 * p]))}, v2u{pk2(lo2(tt[2 * p + 1])), pk2(hi2(tt[2 * p + 1]))});
      }
      st_sc1_128(rws, st_h, hv[0]);
      st_sc1_128(rws, st_h2, hv[1]);
      BAR();
      WAITV(0);
      BAR();
      ST32M(rws, fco, tagv, 255);
      STAMP(27);
    }
  }
  {
    const float bs = blo(b2v);
    h0 += bs;
    h1 += bs;
  }
  f4 acc[4][2];
  v4u bf[2][4];
  u32 rbw = 7 * 16384 + w * 6144 + l * 16, rb1_ = 7 * 16384 + (w ^ 1) * 6144 + l * 16, rb2_ = 7 * 16384 + (w ^ 2) * 6144 + 2048 + l * 16, rb3_ = 7 * 16384 + (w ^ 3) * 6144 + 4096 + l * 16;
  LAUNDER(rbw); LAUNDER(rb1_); LAUNDER(rb2_); LAUNDER(rb3_);
#define GSTEP(MM, n, NW, B, RD, NB, slot, D1, D2)                                 \
  {                                                                               \
    SB();                                                                         \
    if (BST && (n) > 0 && (n) % BST == 0) asm volatile("s_barrier" ::: "memory"); \
    WAITV2(NW, af[n][0], af[n][1]);                                               \
    MM(0, 0, n, B); SB();                                                         \
    if (RD) { NB[0] = trfrag(tr[0] + ((slot)&3) * 16384); } SB();                 \
    MM(0, 1, n, B); SB();                                                         \
    if (RD) { NB[1] = trfrag(tr[1] + ((slot)&3) * 16384); } SB();                 \
    MM(1, 0, n, B); SB();                                                         \
    if (RD) { NB[2] = trfrag(tr[2] + ((slot)&3) * 16384); } SB();                 \
    MM(1, 1, n, B); SB();                                                         \
    if (RD) { NB[3] = trfrag(tr[3] + ((slot)&3) * 16384); } SB();                 \
    MM(2, 0, n, B); SB();                                                         \
    D1; SB();                                                                     \
    MM(2, 1, n, B);                                                               \
    MM(3, 0, n, B); SB();                                                         \
    D2; SB();                                                                     \
    MM(3, 1, n, B); SB();                                                         \
  }
#define GLAST(MM, n, NW, B, r0_, r1_, u0_, DX)                                        \
  {                                                                               \
    SB(); U(u0_)                                                                  \
    WAITV2(NW, af[n][0], af[n][1]); U(u0_ + 1)                                    \
    MM(1, 0, n, B); MM(1, 1, n, B); MM(2, 0, n, B); MM(2, 1, n, B); SB();         \
    *(LAS f4*)(rbw) = acc[1][0]; SB();                                            \
    MM(3, 0, n, B); SB();                                                         \
    *(LAS f4*)(rbw + 1024) = acc[1][1]; SB();                                     \
    MM(3, 1, n, B); SB();                                                         \
    *(LAS f4*)(rbw + 2048) = acc[2][0]; SB();                                     \
    MM(0, 0, n, B); SB();                                                         \
    *(LAS f4*)(rbw + 3072) = acc[2][1]; SB();                                     \
    MM(0, 1, n, B); SB();                                                         \
    *(LAS f4*)(rbw + 4096) = acc[3][0];                                           \
    *(LAS f4*)(rbw + 5120) = acc[3][1];                                           \
    LAUNDER(acc[0][0]); LAUNDER(acc[0][1]); U(u0_ + 2)                            \
    BAR(); U(u0_ + 3)                                                             \
    const f4 p0 = lds128f(rb1_), p2 = lds128f(rb2_), p4_ = lds128f(rb3_), p1 = lds128f(rb1_ + 1024), p3_ = lds128f(rb2_ + 1024), p5 = lds128f(rb3_ + 1024); \
    SB(); DX; SB();                                                                         \
    r0_ = (acc[0][0] + p0) + (p2 + p4_);                                          \
    r1_ = (acc[0][1] + p1) + (p3_ + p5); LAUNDER(r0_); LAUNDER(r1_); U(u0_ + 4)   \
  }
  if (p3) {
    STAMP(9);
    if (wh == 1) { if (NA2 <= 4) w1_issue4(10, 5); w1_issue4(11, 6); w1_issue4(12, 7); w1_issue4(13, 9); w1_issue4(14, 8); w1_issue4(15, 0); }
    TRSET(0)
    u32 abase = WS_HF + ((4 * C + 2 * wh) * 64 + 16 * wq_) * 1024;
    asm volatile("" : "+s"(abase));
    acc[0][0] = acc[0][1] = f4{blo(b1v.x), bhi(b1v.x), blo(b1v.y), bhi(b1v.y)};
    LAUNDER(acc[0][0]); LAUNDER(acc[0][1]);
#pragma unroll
    for (int t = 1; t < 4; ++t) acc[t][0] = acc[t][1] = f4{0, 0, 0, 0};
    STAMP(31);
    if (wh == 0 && SDLY) WAITV(0);
    if ((NOB & 2) || wh == 0) SPOLL8(WS_FLAGC + (FT * 8 + C) * 128 + wq_ * 32)
    if (!(NOB & 2)) BAR();
    STAMP(10);
#define J3(n) ((n) < 2 ? (n) : ((n) <= 11 ? (n) + 4 : (n)-10))
    v4u af[16][2];
#define A3L(n, m) { if ((n) < 16) { const u32 so = abase + KS3(J3((n)&15)) * 1024 + (m)*65536; LD_SC1(af[(n)&15][m], rws, avo, so, 0); } }
#define Y3(n) (2 * (((n) + PD - 1 < 15 ? (n) + PD - 1 : 15) - (n)))
    A3L(0, 0) A3L(0, 1) A3L(1, 0) A3L(1, 1) A3L(2, 0) A3L(2, 1)
    if (PD > 3) { A3L(3, 0) A3L(3, 1) }
    if (PD > 4) { A3L(4, 0) A3L(4, 1) }
    if (PD > 5) { A3L(5, 0) A3L(5, 1) }
    if (PD > 6) { A3L(6, 0) A3L(6, 1) }
    if (LPRIO && wh == 1) __builtin_amdgcn_s_setprio(1);
#define MM3(t, m, n, B) acc[t][m] = mfma32(B[t], af[n][m], acc[t][m])
    GSTEP(MM3, 0, Y3(0), bw[0], 0, bf[0], 0, A3L(0 + PD, 0), A3L(0 + PD, 1))
    BAR();
    STAMP(17);
    GSTEP(MM3, 1, Y3(1), bw[1], 1, bf[0], 1, A3L(1 + PD, 0), A3L(1 + PD, 1))
    GSTEP(MM3, 2, Y3(2), bf[0], 1, bf[1], 2, A3L(2 + PD, 0), A3L(2 + PD, 1))
    GSTEP(MM3, 3, Y3(3), bf[1], 1, bf[0], 3, A3L(3 + PD, 0), A3L(3 + PD, 1))
    TRSET(1)
    GSTEP(MM3, 4, Y3(4), bf[0], 1, bf[1], 4, A3L(4 + PD, 0), A3L(4 + PD, 1))
    GSTEP(MM3, 5, Y3(5), bf[1], 1, bf[0], 5, A3L(5 + PD, 0), A3L(5 + PD, 1))
    GSTEP(MM3, 6, Y3(6), bf[0], 1, bf[1], 6, A3L(6 + PD, 0), A3L(6 + PD, 1))
    GSTEP(MM3, 7, Y3(7), bf[1], 1, bf[0], 7, A3L(7 + PD, 0), A3L(7 + PD, 1))
    TRSET(2)
    GSTEP(MM3, 8, Y3(8), bf[0], 1, bf[1], 9, A3L(8 + PD, 0), A3L(8 + PD, 1))
    GSTEP(MM3, 9, Y3(9), bf[1], 1, bf[0], 8, A3L(9 + PD, 0), A3L(9 + PD, 1))
    TRSET(0)
    GSTEP(MM3, 10, Y3(10), bf[0], 1, bf[1], 0, A3L(10 + PD, 0), A3L(10 + PD, 1))
    GSTEP(MM3, 11, Y3(11), bf[1], 0, bf[0], 0, { A3L(11 + PD, 0) A3L(11 + PD, 1) }, { A3L(12 + PD, 0) A3L(12 + PD, 1) })
    U(0)
    BAR();
    U(1)
    STAMP(18);
    GSTEP(MM3, 12, 6, bw[2], 0, bf[1], 0, w2_issue(0, 0), if (NE > 1) w2_issue(1, 1))
    U(2)
    GSTEP(MM3, 13, 4 + (NE > 1 ? 4 : 2), bw[3], 0, bf[1], 0, if (NE > 2) w2_issue(2, 2), )
    U(3)
    GSTEP(MM3, 14, 2 + 2 * NE, bw[4], 0, bf[1], 0, , )
    STAMP(11);
    f4 u0, u1;
    GLAST(MM3, 15, 2 * NE, bw[5], u0, u1, 4, { if (NE <= 1) w2_issue(1, 1); if (NE <= 2) w2_issue(2, 2); w2_issue(3, 3); if (NF > 4) w2_issue(4, 4); })
    if (LPRIO) __builtin_amdgcn_s_setprio(0);
    auto gelu2 = [&](f2 v) {
      const float k1 = -2.f * 0.7978845608028654f * 1.44269504088896f, k2 = k1 * 0.044715f;
      const f2 z = v * fma2(v * v, bc2(k2), bc2(k1));
      const f2 d = f2{__builtin_amdgcn_exp2f(z.x), __builtin_amdgcn_exp2f(z.y)} + bc2(1.f);
      return pk2(v * f2{__builtin_amdgcn_rcpf(d.x), __builtin_amdgcn_rcpf(d.y)});
    };
    const v2u ua = {gelu2(lo2(u0)), gelu2(hi2(u0))}, ub = {gelu2(lo2(u1)), gelu2(hi2(u1))};
    st_sc1_128(rws, st_u, widen(ua, ub));
    U(9)
    STAMP(19);
    if (NF <= 4) w2_issue(4, 4);
    w2_issue(5, 5); w2_issue(6, 6);
    U(10)
    WAITV(NF > 4 ? 4 : 6);
    U(11)
    {
      u32 fo = WS_FLAGD + ((4 * w + (l & 3)) * 8 + C) * 128 + (R + ((l >> 2) & ((bi >> 2) == 6))) * 4, tv_ = tag;
      LAUNDER(fo); LAUNDER(tv_);
      BAR();
      ST32M(rws, fo, tv_, 255);
    }
    if (b == 0 && tid == 128) st_sc1_32(rws, WS_EPOCH, tag);
  } else {
    WAITV(0);
    BAR();
    _Pragma("nounroll") for (u32 i = 4 * ESW + w; i < SWLIM; i += 8) SWEEP1(i, xcd & 1);
    if (SW2 == 1 && w >= 4 && 7 * (w - 4) < SWLIM) {
      const u32 cb = (3u - (rw2.x >> 7)) & 7u, lm = min(l, 55);
      TOUCH(rw2, ((lm >> 3) * 16 + (lm & 7)) * 4096, (448 * (bi & 3) + 112 * (w - 4) + 8 * (~xcd & 1)) * 4096 + (xcd >> 1) * 1024 + cb * 128);
    }
    if (SW2 > 1) { _Pragma("nounroll") for (u32 i = w; i < SW2; i += 8) SWEEP1(i, ~xcd & 1); }
    w2_issue(0, 0); w2_issue(1, 1); w2_issue(2, 2); w2_issue(3, 3); w2_issue(4, 4); w2_issue(5, 5); w2_issue(6, 6);
    WAITV(0);
    BAR();
  }
  U(12)
  STAMP(12);
  TRSET(0) BREAD(bw[0], 0) BREAD(bw[1], 1) BREAD(bw[2], 2) BREAD(bw[3], 3)
  if (wh == 1) { w2_issue4(7, 7); w2_issue4(8, 8); }
  BAR();
  if (wh == 1) { w2_issue4(9, 9); w2_issue4(10, 0); w2_issue4(11, 1); if (W2PRE > 12) w2_issue4(12, 2); if (W2PRE > 13) w2_issue4(13, 3); }
  {
    u32 abase = WS_UF + ((4 * C + 2 * wh) * 56 + 14 * wq_) * 1024;
    asm volatile("" : "+s"(abase));
    u32 oo = (R * 64 + 16 * wq_ + c) * 1024 + (64 * C + 32 * wh + 16 * (g & 1) + 8 * (g >> 1)) * 2;
    LAUNDER(oo);
    acc[0][0] = h0;
    acc[0][1] = h1;
#pragma unroll
    for (int t = 1; t < 4; ++t) acc[t][0] = acc[t][1] = f4{0, 0, 0, 0};
    STAMP(34);
    if ((NOB & 4) || wh == 0) SPOLL8(WS_FLAGD + (R * 8 + C) * 128 + wq_ * 32)
    if (!(NOB & 4)) BAR();
    STAMP(13);
    v4u af[14][2];
#define A4L(n, m) { if ((n) < 14) { const u32 so = abase + KS4((n) % 14) * 1024 + (m)*56 * 1024; LD_SC1(af[(n) % 14][m], rws, avo, so, 0); } }
#define Y4(n) (2 * (((n) + PD - 1 < 13 ? (n) + PD - 1 : 13) - (n)))
    A4L(0, 0) A4L(0, 1) A4L(1, 0) A4L(1, 1) A4L(2, 0) A4L(2, 1)
    if (PD > 3) { A4L(3, 0) A4L(3, 1) }
    if (PD > 4) { A4L(4, 0) A4L(4, 1) }
    if (PD > 5) { A4L(5, 0) A4L(5, 1) }
    if (PD > 6) { A4L(6, 0) A4L(6, 1) }
    if (LPRIO && wh == 1) __builtin_amdgcn_s_setprio(1);
#define MM4(t, m, n, B) acc[t][m] = mfma32(af[n][m], B[t], acc[t][m])
    GSTEP(MM4, 0, Y4(0), bw[0], 0, bf[0], 0, A4L(0 + PD, 0), A4L(0 + PD, 1))
    GSTEP(MM4, 1, Y4(1), bw[1], 0, bf[0], 0, A4L(1 + PD, 0), A4L(1 + PD, 1))
    GSTEP(MM4, 2, Y4(2), bw[2], 0, bf[0], 0, A4L(2 + PD, 0), A4L(2 + PD, 1))
    BAR();
    STAMP(20);
    TRSET(1)
    GSTEP(MM4, 3, Y4(3), bw[3], 1, bf[0], 4, A4L(3 + PD, 0), A4L(3 + PD, 1))
    GSTEP(MM4, 4, Y4(4), bf[0], 1, bf[1], 5, A4L(4 + PD, 0), A4L(4 + PD, 1))
    GSTEP(MM4, 5, Y4(5), bf[1], 1, bf[0], 6, A4L(5 + PD, 0), A4L(5 + PD, 1))
    GSTEP(MM4, 6, Y4(6), bf[0], 1, bf[1], 7, A4L(6 + PD, 0), A4L(6 + PD, 1))
    TRSET(2)
    GSTEP(MM4, 7, Y4(7), bf[1], 1, bf[0], 8, A4L(7 + PD, 0), A4L(7 + PD, 1))
    GSTEP(MM4, 8, Y4(8), bf[0], 1, bf[1], 9, A4L(8 + PD, 0), A4L(8 + PD, 1))
    BAR();
    TRSET(0)
    GSTEP(MM4, 9, Y4(9), bf[1], 1, bf[0], 0, A4L(9 + PD, 0), A4L(9 + PD, 1))
    GSTEP(MM4, 10, Y4(10), bf[0], 1, bf[1], 1, A4L(10 + PD, 0), A4L(10 + PD, 1))
    GSTEP(MM4, 11, Y4(11), bf[1], 1, bf[0], 2, , )
    GSTEP(MM4, 12, Y4(12), bf[0], 1, bf[1], 3, , )
    STAMP(14);
    f4 y0, y1;
    GLAST(MM4, 13, 0, bf[1], y0, y1, 14, )
    const v2u oa = {pk2(lo2(y0)), pk2(hi2(y0))}, ob = {pk2(lo2(y1)), pk2(hi2(y1))};
    { const v4u ro = mkrs(a.out, S_ * D_ * 2); st_sc1_128(ro, oo, widen(oa, ob)); }
  }
}

extern "C" void kernel_launch(void* const* d_in, const int* in_sizes, int n_in, void* d_out, int out_size, void* d_ws, size_t ws_size, hipStream_t stream) {
  Args a;
  a.x = d_in[0];
  a.wqkv = d_in[1];
  a.wo = d_in[2];
  a.w1 = d_in[4];
  a.b1 = d_in[5];
  a.w2 = d_in[6];
  a.b2 = d_in[7];
  a.out = d_out;
  a.ws = d_ws;
  (void)hipFuncSetAttribute((const void*)fused_kernel, hipFuncAttributeMaxDynamicSharedMemorySize, LDS_BYTES);
  fused_kernel<<<256, 512, LDS_BYTES, stream>>>(a);
}
```
